```python
import torch

def _softmax(x, axis=-1):
    return torch.softmax(x, dim=axis)
def _gelu(x):
    return torch.nn.functional.gelu(x, approximate="tanh")
def _silu(x):
    return torch.nn.functional.silu(x)
def _sigmoid(x):
    return torch.sigmoid(x)
def _layernorm(x, axis=-1, eps=1e-5):
    m = x.mean(dim=axis, keepdim=True)
    v = x.var(dim=axis, keepdim=True, unbiased=False)
    return (x - m) / torch.sqrt(v + eps)
def _scatter_add(tgt, idx, src, axis=0):
    return tgt.clone().index_add_(axis, idx, src)
def _segment_sum(src, seg, n):
    out = torch.zeros((n,) + src.shape[1:], dtype=src.dtype)
    return out.index_add_(0, seg, src)
def _pad(x, pw):
    flat = tuple(v for pair in reversed(pw) for v in pair)
    return torch.nn.functional.pad(x, flat)

kernel_name = 'standalone__dcn_v2_cross__recsys'

def setup_inputs():

    _w = (lambda a: (lambda t: t.to(torch.bfloat16) if t.is_floating_point() else t)(torch.as_tensor(a)))
    B, F, D, L = 1024, 100, 64, 3
    Din = min(F * D, 1024)
    return {
        "x0": _w((lambda *s: torch.randn(*s, dtype=torch.bfloat16) * 0.05)(B, Din)),
        "ws": _w((lambda *s: torch.randn(*s, dtype=torch.bfloat16) * 0.05)(L, Din, Din)),
        "bs": _w((lambda *s: torch.randn(*s, dtype=torch.bfloat16) * 0.05)(L, Din)),
    }

def reference(x0, ws, bs):
    x = x0
    for l in range(ws.shape[0]):
        x = x0 * (x @ ws[l] + bs[l]) + x
    return x
```

```cpp
#include <hip/hip_runtime.h>
#include <hip/hip_bf16.h>

typedef short bf16x8 __attribute__((ext_vector_type(8)));
typedef short bf16x4 __attribute__((ext_vector_type(4)));
typedef float f32x16 __attribute__((ext_vector_type(16)));
typedef unsigned int u32;
typedef unsigned short u16;
#define LAS __attribute__((address_space(3)))
#define GAS __attribute__((address_space(1)))

constexpr int D = 1024;
constexpr int BK = 64;
constexpr int NST = 4;
constexpr int STAGE_B = 16384;

__device__ __forceinline__ float bf2f(u16 v) { return __builtin_bit_cast(float, (u32)v << 16); }
__device__ __forceinline__ float rbf(float f) { return (float)(__bf16)f; }
__device__ __forceinline__ u16 f2bf(float f) { return __builtin_bit_cast(u16, (__bf16)f); }

__global__ void __launch_bounds__(256, 1)
layer_kernel(const u16* __restrict__ x0, const u16* __restrict__ x, const u16* __restrict__ w,
             const u16* __restrict__ b, u16* __restrict__ out) {
  extern __shared__ __attribute__((aligned(16))) char smem[];
  const int tid = threadIdx.x, lane = tid & 63;
  const int wv = __builtin_amdgcn_readfirstlane(tid >> 6);
  const int bid = blockIdx.x;
  const int xg = bid & 7, j = bid >> 3;
  const int rp = (xg >> 1) * 4 + (j >> 3), cs = (xg & 1) * 8 + (j & 7);
  const int m0 = rp * 64, n0 = cs * 64;
  const int wm = wv >> 1, wn = wv & 1;

  const int sr = lane >> 3, sc = lane & 7;
  const u16* ga[2]; const u16* gb[2];
#pragma unroll
  for (int i = 0; i < 2; ++i) {
    const int r = wv * 16 + i * 8 + sr;
    ga[i] = x + (size_t)(m0 + r) * D + ((sc ^ ((r >> 1) & 7)) << 3);
    gb[i] = w + (size_t)r * D + n0 + ((sc ^ (((r >> 1) & 1) << 2)) << 3);
  }
  auto stage = [&](int t) {
    char* s = smem + (t % NST) * STAGE_B + wv * 2048;
#pragma unroll
    for (int i = 0; i < 2; ++i) {
      __builtin_amdgcn_global_load_lds((const GAS void*)(ga[i] + t * BK), (LAS void*)(s + i * 1024), 16, 0, 0);
      __builtin_amdgcn_global_load_lds((const GAS void*)(gb[i] + (size_t)t * BK * D), (LAS void*)(s + 8192 + i * 1024), 16, 0, 0);
    }
  };
  f32x16 acc = {};
  const int ar = wm * 32 + (lane & 31), h = lane >> 5;
  const u32 sbase = (u32)(size_t)(LAS char*)smem;
  const int q = (lane & 15) >> 2, p = lane & 3, blk = (lane >> 4) & 1;
  constexpr int NT = D / BK;
  stage(0); stage(1); stage(2);
  for (int t = 0; t < NT; ++t) {
    if (t + 2 < NT) asm volatile("s_waitcnt vmcnt(8)" ::: "memory");
    else if (t + 1 < NT) asm volatile("s_waitcnt vmcnt(4)" ::: "memory");
    else asm volatile("s_waitcnt vmcnt(0)" ::: "memory");
    __builtin_amdgcn_s_barrier();
    if (t + 3 < NT) stage(t + 3);
    const u32 sa = sbase + (t % NST) * STAGE_B, sb = sa + 8192;
    bf16x8 af[4]; bf16x4 bl[4], bh[4];
#pragma unroll
    for (int ks = 0; ks < 4; ++ks) {
      const u32 aaddr = sa + ar * 128 + (((ks * 2 + h) ^ ((ar >> 1) & 7)) << 4);
      asm volatile("ds_read_b128 %0, %1" : "=v"(af[ks]) : "v"(aaddr));
      const int k0 = ks * 16 + h * 8 + q;
      const int cch = wn * 4 + blk * 2 + (p >> 1);
      const u32 b0 = sb + k0 * 128 + ((cch ^ (((k0 >> 1) & 1) << 2)) << 4) + (p & 1) * 8;
      asm volatile("ds_read_b64_tr_b16 %0, %1" : "=v"(bl[ks]) : "v"(b0));
      asm volatile("ds_read_b64_tr_b16 %0, %1 offset:512" : "=v"(bh[ks]) : "v"(b0));
    }
    asm volatile("s_waitcnt lgkmcnt(0)" : "+v"(af[0]), "+v"(af[1]), "+v"(af[2]), "+v"(af[3]), "+v"(bl[0]), "+v"(bl[1]), "+v"(bl[2]), "+v"(bl[3]),
                 "+v"(bh[0]), "+v"(bh[1]), "+v"(bh[2]), "+v"(bh[3]) :: "memory");
#pragma unroll
    for (int ks = 0; ks < 4; ++ks) {
      const bf16x8 bf = __builtin_shufflevector(bl[ks], bh[ks], 0, 1, 2, 3, 4, 5, 6, 7);
      acc = __builtin_amdgcn_mfma_f32_32x32x16_bf16(af[ks], bf, acc, 0, 0, 0);
    }
  }
  const int col = n0 + wn * 32 + (lane & 31);
  const float bias = bf2f(b[col]);
#pragma unroll
  for (int r = 0; r < 16; ++r) {
    const int row = m0 + wm * 32 + (r & 3) + 8 * (r >> 2) + 4 * h;
    const size_t o = (size_t)row * D + col;
    float t = rbf(acc[r]);
    t = rbf(t + bias);
    t = rbf(bf2f(x0[o]) * t);
    out[o] = f2bf(t + bf2f(x[o]));
  }
}

extern "C" void kernel_launch(void* const* d_in, const int* in_sizes, int n_in, void* d_out, int out_size,
                              void* d_ws, size_t ws_size, hipStream_t stream) {
  const u16* x0 = (const u16*)d_in[0];
  const u16* ws = (const u16*)d_in[1];
  const u16* bs = (const u16*)d_in[2];
  u16* t1 = (u16*)d_ws;
  u16* t2 = t1 + D * D;
  const int lds = NST * STAGE_B;
  layer_kernel<<<256, 256, lds, stream>>>(x0, x0, ws, bs, t1);
  layer_kernel<<<256, 256, lds, stream>>>(x0, t1, ws + D * D, bs + D, t2);
  layer_kernel<<<256, 256, lds, stream>>>(x0, t2, ws + 2 * D * D, bs + 2 * D, (u16*)d_out);
}
```

```cpp
#include <hip/hip_runtime.h>
#include <hip/hip_bf16.h>

typedef short bf16x8 __attribute__((ext_vector_type(8)));
typedef short bf16x4 __attribute__((ext_vector_type(4)));
typedef float f32x16 __attribute__((ext_vector_type(16)));
typedef unsigned int u32;
typedef unsigned short u16;
#define LAS __attribute__((address_space(3)))
#define GAS __attribute__((address_space(1)))

constexpr int D = 1024;
constexpr int BK = 64;
constexpr int NST = 4;
constexpr int STAGE_B = 16384;

__device__ __forceinline__ float bf2f(u16 v) { return __builtin_bit_cast(float, (u32)v << 16); }
__device__ __forceinline__ float rbf(float f) { return (float)(__bf16)f; }
__device__ __forceinline__ u16 f2bf(float f) { return __builtin_bit_cast(u16, (__bf16)f); }

__global__ void __launch_bounds__(256, 1)
layer_kernel(const u16* __restrict__ x0, const u16* __restrict__ x, const u16* __restrict__ w,
             const u16* __restrict__ b, u16* __restrict__ out) {
  extern __shared__ __attribute__((aligned(16))) char smem[];
  const int tid = threadIdx.x, lane = tid & 63;
  const int wv = __builtin_amdgcn_readfirstlane(tid >> 6);
  const int bid = blockIdx.x;
  const int xg = bid & 7, j = bid >> 3;
  const int rp = (xg >> 1) * 4 + (j >> 3), cs = (xg & 1) * 8 + (j & 7);
  const int m0 = rp * 64, n0 = cs * 64;
  const int wm = wv >> 1, wn = wv & 1;

  const int sr = lane >> 3, sc = lane & 7;
  const u16* ga[2]; const u16* gb[2];
#pragma unroll
  for (int i = 0; i < 2; ++i) {
    const int r = wv * 16 + i * 8 + sr;
    ga[i] = x + (size_t)(m0 + r) * D + ((sc ^ ((r >> 1) & 7)) << 3);
    gb[i] = w + (size_t)r * D + n0 + ((sc ^ (((r >> 1) & 1) << 2)) << 3);
  }
  auto stage = [&](int t) {
    char* s = smem + (t % NST) * STAGE_B + wv * 2048;
#pragma unroll
    for (int i = 0; i < 2; ++i) {
      __builtin_amdgcn_global_load_lds((const GAS void*)(ga[i] + t * BK), (LAS void*)(s + i * 1024), 16, 0, 0);
      __builtin_amdgcn_global_load_lds((const GAS void*)(gb[i] + (size_t)t * BK * D), (LAS void*)(s + 8192 + i * 1024), 16, 0, 0);
    }
  };
  f32x16 acc = {};
  const int ar = wm * 32 + (lane & 31), h = lane >> 5;
  const u32 sbase = (u32)(size_t)(LAS char*)smem;
  const int q = (lane & 15) >> 2, p = lane & 3, blk = (lane >> 4) & 1;
  constexpr int NT = D / BK;
  stage(0); stage(1); stage(2);
  for (int t = 0; t < NT; ++t) {
    if (t + 2 < NT) asm volatile("s_waitcnt vmcnt(8)" ::: "memory");
    else if (t + 1 < NT) asm volatile("s_waitcnt vmcnt(4)" ::: "memory");
    else asm volatile("s_waitcnt vmcnt(0)" ::: "memory");
    __builtin_amdgcn_s_barrier();
    if (t + 3 < NT) stage(t + 3);
    const u32 sa = sbase + (t % NST) * STAGE_B, sb = sa + 8192;
    bf16x8 af[4]; bf16x4 bl[4], bh[4];
#pragma unroll
    for (int ks = 0; ks < 4; ++ks) {
      const u32 aaddr = sa + ar * 128 + (((ks * 2 + h) ^ ((ar >> 1) & 7)) << 4);
      asm volatile("ds_read_b128 %0, %1" : "=v"(af[ks]) : "v"(aaddr));
      const int k0 = ks * 16 + h * 8 + q;
      const int cch = wn * 4 + blk * 2 + (p >> 1);
      const u32 b0 = sb + k0 * 128 + ((cch ^ (((k0 >> 1) & 1) << 2)) << 4) + (p & 1) * 8;
      asm volatile("ds_read_b64_tr_b16 %0, %1" : "=v"(bl[ks]) : "v"(b0));
      asm volatile("ds_read_b64_tr_b16 %0, %1 offset:512" : "=v"(bh[ks]) : "v"(b0));
    }
    asm volatile("s_waitcnt lgkmcnt(0)" : "+v"(af[0]), "+v"(af[1]), "+v"(af[2]), "+v"(af[3]), "+v"(bl[0]), "+v"(bl[1]), "+v"(bl[2]), "+v"(bl[3]),
                 "+v"(bh[0]), "+v"(bh[1]), "+v"(bh[2]), "+v"(bh[3]) :: "memory");
#pragma unroll
    for (int ks = 0; ks < 4; ++ks) {
      const bf16x8 bf = __builtin_shufflevector(bl[ks], bh[ks], 0, 1, 2, 3, 4, 5, 6, 7);
      acc = __builtin_amdgcn_mfma_f32_32x32x16_bf16(af[ks], bf, acc, 0, 0, 0);
    }
  }
  const int col = n0 + wn * 32 + (lane & 31);
  const float bias = bf2f(b[col]);
#pragma unroll
  for (int r = 0; r < 16; ++r) {
    const int row = m0 + wm * 32 + (r & 3) + 8 * (r >> 2) + 4 * h;
    const size_t o = (size_t)row * D + col;
    float t = rbf(acc[r]);
    t = rbf(t + bias);
    t = rbf(bf2f(x0[o]) * t);
    out[o] = f2bf(t + bf2f(x[o]));
  }
}


__global__ void probe_kernel(const u32* __restrict__ p, u32* __restrict__ sink, int n, int stride) {
  u32 off = 0, acc = 0;
  for (int i = 0; i < n; ++i) {
    u32 v;
    asm volatile("global_load_dword %0, %1, %2\n\ts_waitcnt vmcnt(0)" : "=&v"(v) : "v"(off), "s"(p) : "memory");
    acc ^= v;
    off += stride;
  }
  if (threadIdx.x == 0) sink[0] = acc;
}

extern "C" void kernel_launch(void* const* d_in, const int* in_sizes, int n_in, void* d_out, int out_size,
                              void* d_ws, size_t ws_size, hipStream_t stream) {
  const u16* x0 = (const u16*)d_in[0];
  const u16* ws = (const u16*)d_in[1];
  const u16* bs = (const u16*)d_in[2];
  u16* t1 = (u16*)d_ws;
  u16* t2 = t1 + D * D;
  const int lds = NST * STAGE_B;
  probe_kernel<<<1, 64, 0, stream>>>((const u32*)ws, (u32*)d_ws + (8<<20), 0, 4224);
  layer_kernel<<<256, 256, lds, stream>>>(x0, x0, ws, bs, t1);
  layer_kernel<<<256, 256, lds, stream>>>(x0, t1, ws + D * D, bs + D, t2);
  layer_kernel<<<256, 256, lds, stream>>>(x0, t2, ws + 2 * D * D, bs + 2 * D, (u16*)d_out);
}
```

```cpp
#include <hip/hip_runtime.h>
#include <hip/hip_bf16.h>

typedef short bf16x8 __attribute__((ext_vector_type(8)));
typedef short bf16x4 __attribute__((ext_vector_type(4)));
typedef float f32x16 __attribute__((ext_vector_type(16)));
typedef unsigned int u32;
typedef unsigned short u16;
typedef unsigned long long u64;
typedef u32 v2u __attribute__((ext_vector_type(2)));
#define LAS __attribute__((address_space(3)))
#define GAS __attribute__((address_space(1)))
#define RLX_AGENT __ATOMIC_RELAXED, __HIP_MEMORY_SCOPE_AGENT

constexpr int D = 1024;
constexpr int BK = 64;
constexpr int NST = 8;
constexpr int STAGE_B = 16384;
constexpr u32 SPIN_CAP = 1u << 16;

__device__ __forceinline__ float lo2f(u32 v) { return __builtin_bit_cast(float, v << 16); }
__device__ __forceinline__ float hi2f(u32 v) { return __builtin_bit_cast(float, v & 0xffff0000u); }
__device__ __forceinline__ float rbf(float f) { return (float)(__bf16)f; }
__device__ __forceinline__ u32 f2bf(float f) { return __builtin_bit_cast(u16, (__bf16)f); }
__device__ __forceinline__ float cross(float acc, float bias, float x0, float x) { return rbf(x0 * rbf(rbf(acc) + bias)) + x; }

__global__ void __launch_bounds__(256, 1)
fused_kernel(const u16* __restrict__ x0, const u16* __restrict__ wsp, const u16* __restrict__ bs, u16* __restrict__ out, char* __restrict__ wk) {
  extern __shared__ __attribute__((aligned(16))) char smem[];
  const int tid = threadIdx.x, lane = tid & 63;
  const int wv = __builtin_amdgcn_readfirstlane(tid >> 6);
  const int bid = blockIdx.x;
  const int xg = bid & 7, j = bid >> 3;
  const int rp = (xg >> 1) * 4 + (j >> 3), cs = (xg & 1) * 8 + (j & 7);
  const int m0 = rp * 64, n0 = cs * 64;
  const int wm = wv >> 1, wn = wv & 1;
  GAS u32* ctl = (GAS u32*)(wk + (4 << 20));
  GAS u32* mailbox = ctl + rp * 64;
  GAS u32* inbox = ctl + 4096 + rp * 16 * 64;
  u32 nonce = 0, mb = 0;
  if (wv == 0) {
    u32 old = 0;
    if (lane < 16) old = __hip_atomic_load(inbox + cs * 64 + lane, RLX_AGENT);
    else if (lane == 16) old = __hip_atomic_load(mailbox + cs, RLX_AGENT);
    const u64 m = __builtin_amdgcn_ballot_w64(lane < 17 && ((old >> (8 + lane)) & 1u) == 0u);
    nonce = (u32)m << 8;
    if (lane == 0) __hip_atomic_store(mailbox + cs, nonce, RLX_AGENT);
  }

  const int sr = lane >> 3, sc = lane & 7;
  const int ar = wm * 32 + (lane & 31), h = lane >> 5;
  const u32 sbase = (u32)(size_t)(LAS char*)smem;
  const int q = (lane & 15) >> 2, p = lane & 3, blk = (lane >> 4) & 1;
  constexpr int NT = D / BK;
  const int row = m0 + wm * 32 + (lane & 31);
  v2u x0t[4], xt[4];
#pragma unroll
  for (int g = 0; g < 4; ++g) { x0t[g] = *(const GAS v2u*)(x0 + (size_t)row * D + n0 + wn * 32 + 8 * g + 4 * h); xt[g] = x0t[g]; }

  for (int l = 0; l < 3; ++l) {
    const u16* x = l == 0 ? x0 : (const u16*)(wk + (size_t)(l - 1) * (2 << 20));
    u16* y = l == 2 ? out : (u16*)(wk + (size_t)l * (2 << 20));
    const u16* w = wsp + (size_t)l * D * D;
    const u16* ga[2]; const u16* gb[2];
#pragma unroll
    for (int i = 0; i < 2; ++i) {
      const int r = wv * 16 + i * 8 + sr;
      ga[i] = x + (size_t)(m0 + r) * D + ((sc ^ ((r >> 1) & 7)) << 3);
      gb[i] = w + (size_t)r * D + n0 + ((sc ^ (((r >> 1) & 1) << 2)) << 3);
    }
    auto stageA = [&](int t) {
      char* s = smem + (t % NST) * STAGE_B + wv * 2048;
#pragma unroll
      for (int i = 0; i < 2; ++i) __builtin_amdgcn_global_load_lds((const GAS void*)(ga[i] + t * BK), (LAS void*)(s + i * 1024), 16, 0, 0);
    };
    auto stageB = [&](int t) {
      char* s = smem + (t % NST) * STAGE_B + wv * 2048;
#pragma unroll
      for (int i = 0; i < 2; ++i) __builtin_amdgcn_global_load_lds((const GAS void*)(gb[i] + (size_t)t * BK * D), (LAS void*)(s + 8192 + i * 1024), 16, 0, 0);
    };
    v2u bia[4];
#pragma unroll
    for (int g = 0; g < 4; ++g) bia[g] = *(const GAS v2u*)(bs + l * D + n0 + wn * 32 + 8 * g + 4 * h);
    for (int t = 0; t < NST - 1; ++t) stageB(t);
    if (l > 0) {
      if (wv == 0) {
        for (u32 spins = 0; spins < SPIN_CAP; ++spins) {
          u32 v = nonce | 255u, mn = mb;
          if (lane < 16) { v = __hip_atomic_load(inbox + cs * 64 + lane, RLX_AGENT); if (l == 1) mn = __hip_atomic_load(mailbox + lane, RLX_AGENT); }
          if (mn != mb) { mb = mn; __hip_atomic_store(inbox + lane * 64 + cs, (mb & ~255u) | (u32)l, RLX_AGENT); }
          if (__all((v & ~255u) == nonce && (v & 255u) >= (u32)l)) break;
        }
        if (l == 1) {
          asm volatile("s_waitcnt vmcnt(0)" ::: "memory");
          u32 mn = mb;
          if (lane < 16) mn = __hip_atomic_load(mailbox + lane, RLX_AGENT);
          if (mn != mb) { mb = mn; __hip_atomic_store(inbox + lane * 64 + cs, (mb & ~255u) | (u32)l, RLX_AGENT); }
        }
        __builtin_amdgcn_fence(__ATOMIC_ACQUIRE, "agent");
        asm volatile("s_waitcnt vmcnt(0)" ::: "memory");
      }
      __builtin_amdgcn_s_barrier();
    }
    for (int t = 0; t < NST - 1; ++t) stageA(t);
    f32x16 acc = {};
#pragma unroll
    for (int t = 0; t < NT; ++t) {
      if (t == 0) asm volatile("s_waitcnt vmcnt(12)" ::: "memory");
      else if (t == 1) asm volatile("s_waitcnt vmcnt(14)" ::: "memory");
      else if (t == 2) asm volatile("s_waitcnt vmcnt(16)" ::: "memory");
      else if (t == 3) asm volatile("s_waitcnt vmcnt(18)" ::: "memory");
      else if (t == 4) asm volatile("s_waitcnt vmcnt(20)" ::: "memory");
      else if (t == 5) asm volatile("s_waitcnt vmcnt(22)" ::: "memory");
      else if (t <= 9) asm volatile("s_waitcnt vmcnt(24)" ::: "memory");
      else if (t == 10) asm volatile("s_waitcnt vmcnt(20)" ::: "memory");
      else if (t == 11) asm volatile("s_waitcnt vmcnt(16)" ::: "memory");
      else if (t == 12) asm volatile("s_waitcnt vmcnt(12)" ::: "memory");
      else if (t == 13) asm volatile("s_waitcnt vmcnt(8)" ::: "memory");
      else if (t == 14) asm volatile("s_waitcnt vmcnt(4)" ::: "memory");
      else asm volatile("s_waitcnt vmcnt(0)" ::: "memory");
      __builtin_amdgcn_s_barrier();
      if (t + NST - 1 < NT) { stageB(t + NST - 1); stageA(t + NST - 1); }
      const u32 sa = sbase + (t % NST) * STAGE_B, sb = sa + 8192;
      bf16x8 af[4]; bf16x4 bl[4], bh[4];
#pragma unroll
      for (int ks = 0; ks < 4; ++ks) {
        const u32 aaddr = sa + ar * 128 + (((ks * 2 + h) ^ ((ar >> 1) & 7)) << 4);
        asm volatile("ds_read_b128 %0, %1" : "=v"(af[ks]) : "v"(aaddr));
        const int k0 = ks * 16 + h * 8 + q;
        const int cch = wn * 4 + blk * 2 + (p >> 1);
        const u32 b0 = sb + k0 * 128 + ((cch ^ (((k0 >> 1) & 1) << 2)) << 4) + (p & 1) * 8;
        asm volatile("ds_read_b64_tr_b16 %0, %1" : "=v"(bl[ks]) : "v"(b0));
        asm volatile("ds_read_b64_tr_b16 %0, %1 offset:512" : "=v"(bh[ks]) : "v"(b0));
      }
      asm volatile("s_waitcnt lgkmcnt(0)" : "+v"(af[0]), "+v"(af[1]), "+v"(af[2]), "+v"(af[3]), "+v"(bl[0]), "+v"(bl[1]), "+v"(bl[2]), "+v"(bl[3]),
                   "+v"(bh[0]), "+v"(bh[1]), "+v"(bh[2]), "+v"(bh[3]) :: "memory");
#pragma unroll
      for (int ks = 0; ks < 4; ++ks) {
        const bf16x8 bf = __builtin_shufflevector(bl[ks], bh[ks], 0, 1, 2, 3, 4, 5, 6, 7);
        acc = __builtin_amdgcn_mfma_f32_32x32x16_bf16(bf, af[ks], acc, 0, 0, 0);
      }
    }
    if (l == 0 && wv == 0 && lane < 16) mb = __hip_atomic_load(mailbox + lane, RLX_AGENT);
#pragma unroll
    for (int g = 0; g < 4; ++g) {
      v2u o;
      o.x = f2bf(cross(acc[4 * g], lo2f(bia[g].x), lo2f(x0t[g].x), lo2f(xt[g].x))) | (f2bf(cross(acc[4 * g + 1], hi2f(bia[g].x), hi2f(x0t[g].x), hi2f(xt[g].x))) << 16);
      o.y = f2bf(cross(acc[4 * g + 2], lo2f(bia[g].y), lo2f(x0t[g].y), lo2f(xt[g].y))) | (f2bf(cross(acc[4 * g + 3], hi2f(bia[g].y), hi2f(x0t[g].y), hi2f(xt[g].y))) << 16);
      xt[g] = o;
      GAS u64* dst = (GAS u64*)(y + (size_t)row * D + n0 + wn * 32 + 8 * g + 4 * h);
      const u64 val = (u64)o.x | ((u64)o.y << 32);
      if (l < 2) __hip_atomic_store(dst, val, RLX_AGENT); else *dst = val;
    }
    if (l < 2) {
      asm volatile("s_waitcnt vmcnt(0)" ::: "memory");
      __builtin_amdgcn_s_barrier();
      if (wv == 0 && lane < 16) __hip_atomic_store(inbox + lane * 64 + cs, (mb & ~255u) | (u32)(l + 1), RLX_AGENT);
    }
  }
}

extern "C" void kernel_launch(void* const* d_in, const int* in_sizes, int n_in, void* d_out, int out_size,
                              void* d_ws, size_t ws_size, hipStream_t stream) {
  const int lds = NST * STAGE_B;
  (void)hipFuncSetAttribute((const void*)fused_kernel, hipFuncAttributeMaxDynamicSharedMemorySize, lds);
  fused_kernel<<<256, 256, lds, stream>>>((const u16*)d_in[0], (const u16*)d_in[1], (const u16*)d_in[2], (u16*)d_out, (char*)d_ws);
}
```

```cpp
#include <hip/hip_runtime.h>

__global__ void __launch_bounds__(256, 1) dcn_cross_kernel(const void* x0, const void* w, const void* b, void* out, void* ws) {
  asm volatile(
      "s_load_dwordx2 s[16:17], %0, 0x0\n"
      "s_getpc_b64 s[14:15]\n"
      "L_pc:\n"
      "v_lshlrev_b32 v3, 7, %2\n"
      "v_min_u32 v3, L_hot_end-L_pc, v3\n"
      "global_load_dword v63, v3, s[14:15]\n"
      "s_load_dwordx2 s[20:21], %0, 0x8\n"
      "s_load_dwordx2 s[24:25], %0, 0x10\n"
      "s_load_dwordx2 s[28:29], %0, 0x18\n"
      "s_load_dwordx2 s[64:65], %0, 0x20\n"
      "v_lshrrev_b32 v1, 6, %2\n"
      "v_and_b32 v2, 63, %2\n"
      "v_readfirstlane_b32 s36, v1\n"
      "s_and_b32 s51, %1, 7\n"
      "s_lshr_b32 s60, %1, 3\n"
      "s_lshr_b32 s59, s51, 1\n"
      "s_and_b32 s51, s51, 1\n"
      "s_lshr_b32 s61, s60, 3\n"
      "s_and_b32 s53, s60, 7\n"
      "s_lshl_b32 s37, s59, 2\n"
      "s_add_u32 s37, s37, s61\n"
      "s_lshl_b32 s38, s51, 3\n"
      "s_add_u32 s38, s38, s53\n"
      "s_mul_i32 s48, s53, 8\n"
      "s_mul_i32 s54, s59, 8\n"
      "s_add_u32 s48, s48, s54\n"
      "s_mul_i32 s54, s51, 4\n"
      "s_add_u32 s48, s48, s54\n"
      "s_and_b32 s48, s48, 31\n"
      "s_and_b32 s44, s36, 1\n"
      "s_lshr_b32 s45, s36, 1\n"
      "s_lshl_b32 s51, s45, 16\n"
      "s_lshl_b32 s52, s44, 10\n"
      "s_add_u32 s39, s51, s52\n"
      "s_lshl_b32 s52, s44, 14\n"
      "s_add_u32 s34, s51, s52\n"
      "s_add_u32 s34, s34, 0x8000\n"
      "v_lshrrev_b32 v40, 3, v2\n"
      "v_and_b32 v41, 7, v2\n"
      "v_bfe_u32 v45, v40, 1, 1\n"
      "v_lshlrev_b32 v45, 2, v45\n"
      "v_xor_b32 v45, v45, v41\n"
      "v_lshlrev_b32 v45, 4, v45\n"
      "v_lshl_add_u32 v45, v40, 11, v45\n"
      "s_lshl_b32 s51, s45, 20\n"
      "s_add_u32 s51, s51, s52\n"
      "s_lshl_b32 s54, s38, 7\n"
      "s_add_u32 s51, s51, s54\n"
      "v_add_u32 v1, s51, v45\n"
      "v_lshrrev_b32 v39, 1, v40\n"
      "v_xor_b32 v39, v39, v41\n"
      "v_lshlrev_b32 v39, 4, v39\n"
      "v_lshl_add_u32 v39, v40, 11, v39\n"
      "s_lshl_b32 s51, s37, 1\n"
      "s_add_u32 s51, s51, s44\n"
      "s_lshl_b32 s33, s51, 16\n"
      "s_lshl_b32 s52, s45, 10\n"
      "s_add_u32 s52, s52, s33\n"
      "v_add_u32 v4, s52, v39\n"
      "v_xor_b32 v46, 64, v4\n"
      "v_add_u32 v5, 0x3c00, v46\n"
      "v_add_u32 v6, 0x7800, v4\n"
      "v_add_u32 v7, 0xb400, v46\n"
      "s_mov_b32 s18, 0x200000\n"
      "s_mov_b32 s19, 0x20000\n"
      "s_mov_b32 s22, 0x600000\n"
      "s_mov_b32 s23, 0x20000\n"
      "s_waitcnt lgkmcnt(0)\n"
      "s_and_b32 s17, s17, 0xffff\n"
      "s_and_b32 s21, s21, 0xffff\n"
      "s_add_u32 m0, s34, 0\n"
      "s_lshl_b32 s42, s48, 5\n"
      "buffer_load_dwordx4 v4, s[16:19], s42 offen offset:0  lds\n"
      "buffer_load_dwordx4 v5, s[16:19], s42 offen offset:1024  lds\n"
      "buffer_load_dwordx4 v6, s[16:19], s42 offen offset:2048  lds\n"
      "buffer_load_dwordx4 v7, s[16:19], s42 offen offset:3072  lds\n"
      "v_and_b32 v38, 15, v2\n"
      "s_lshl_b32 s51, s37, 8\n"
      "s_lshl_b32 s52, s38, 2\n"
      "s_add_u32 s51, s51, s52\n"
      "s_add_u32 s51, s51, 0x400000\n"
      "v_mov_b32 v28, s51\n"
      "s_lshl_b32 s51, s37, 4\n"
      "s_add_u32 s51, s51, s38\n"
      "s_lshl_b32 s51, s51, 8\n"
      "s_add_u32 s51, s51, 0x410000\n"
      "v_lshl_add_u32 v25, v38, 2, s51\n"
      "v_cmp_gt_u32 vcc, 16, v2\n"
      "v_lshlrev_b32 v30, 11, v40\n"
      "s_lshl_b32 s52, s59, 3\n"
      "v_cndmask_b32 v27, v28, v25, vcc\n"
      "s_add_u32 s52, s52, s60\n"
      "s_and_b32 s52, s52, 31\n"
      "s_lshl_b32 s52, s52, 2\n"
      "s_add_u32 s52, s52, s36\n"
      "s_lshl_b32 s63, s52, 14\n"
      "s_and_b32 s52, s38, 8\n"
      "s_lshl_b32 s52, s52, 7\n"
      "s_add_u32 s63, s63, s52\n"
      "s_mov_b32 s66, 0x420000\n"
      "s_mov_b32 s67, 0x20000\n"
      "s_and_b32 s25, s25, 0xffff\n"
      "s_and_b32 s29, s29, 0xffff\n"
      "s_and_b32 s65, s65, 0xffff\n"
      "s_cmp_lg_u32 s36, 0\n"
      "s_cbranch_scc1 L_h0\n"
      "buffer_load_dword v29, v27, s[64:67], 0 offen sc1\n"
      "L_h0:\n"
      "s_lshr_b32 s51, s20, 7\n"
      "s_sub_u32 s51, 3, s51\n"
      "s_and_b32 s51, s51, 7\n"
      "s_lshl_b32 s51, s51, 7\n"
      "s_add_u32 s63, s63, s51\n"
      "buffer_load_dword v0, v30, s[20:23], s63 offen\n"
      "s_cmp_lt_u32 s61, 2\n"
      "s_cbranch_scc1 L_fq1\n"
      "s_and_b32 s52, s38, 7\n"
      "s_mul_i32 s52, s52, 8\n"
      "s_sub_u32 s52, s48, s52\n"
      "s_bfe_u32 s51, s60, 0x20002\n"
      "s_add_u32 s52, s52, s51\n"
      "s_add_u32 s52, s52, 4\n"
      "v_bfe_u32 v46, v2, 2, 3\n"
      "v_mad_u32_u24 v47, v46, 8, s52\n"
      "v_lshrrev_b32 v45, 5, v2\n"
      "v_lshl_add_u32 v46, v45, 4, v46\n"
      "v_lshlrev_b32 v46, 7, v46\n"
      "s_bfe_u32 s51, s60, 0x10001\n"
      "s_lshl_b32 s51, s51, 20\n"
      "s_and_b32 s52, s60, 1\n"
      "s_lshl_b32 s52, s52, 2\n"
      "s_add_u32 s52, s52, s36\n"
      "s_lshl_b32 s52, s52, 12\n"
      "s_add_u32 s51, s51, s52\n"
      "s_and_b32 s52, s38, 8\n"
      "s_lshl_b32 s52, s52, 7\n"
      "s_add_u32 s51, s51, s52\n"
      "v_and_b32 v45, 31, v47\n"
      "v_lshl_add_u32 v45, v45, 15, v46\n"
      "buffer_load_dword v0, v45, s[20:23], s51 offen\n"
      "v_add_u32 v47, 4, v47\n"
      "L_fq1:\n"
      "s_add_u32 m0, s39, 0\n"
      "s_lshl_b32 s40, s48, 15\n"
      "buffer_load_dwordx4 v1, s[20:23], s40 offen  lds\n"
      "s_add_u32 m0, s34, 4096\n"
      "s_add_u32 s42, s42, 128\n"
      "s_and_b32 s42, s42, 0x3ff\n"
      "buffer_load_dwordx4 v4, s[16:19], s42 offen offset:0  lds\n"
      "s_add_u32 m0, s39, 2048\n"
      "s_add_u32 s40, s40, 0x8000\n"
      "s_and_b32 s40, s40, 0xfffff\n"
      "buffer_load_dwordx4 v1, s[20:23], s40 offen  lds\n"
      "s_add_u32 m0, s34, 4096\n"
      "s_nop 0\n"
      "buffer_load_dwordx4 v5, s[16:19], s42 offen offset:1024  lds\n"
      "s_add_u32 m0, s39, 4096\n"
      "s_add_u32 s40, s40, 0x8000\n"
      "s_and_b32 s40, s40, 0xfffff\n"
      "buffer_load_dwordx4 v1, s[20:23], s40 offen  lds\n"
      "s_add_u32 m0, s34, 4096\n"
      "s_nop 0\n"
      "buffer_load_dwordx4 v6, s[16:19], s42 offen offset:2048  lds\n"
      "s_and_b32 s53, s38, 7\n"
      "s_lshr_b32 s51, s38, 3\n"
      "s_mul_i32 s49, s53, 2\n"
      "s_mul_i32 s54, s61, 30\n"
      "s_add_u32 s49, s49, s54\n"
      "s_mul_i32 s54, s59, 8\n"
      "s_add_u32 s49, s49, s54\n"
      "s_mul_i32 s54, s51, 16\n"
      "s_add_u32 s49, s49, s54\n"
      "s_and_b32 s49, s49, 31\n"
      "s_mov_b32 s50, s49\n"
      "v_and_b32 v42, 31, v2\n"
      "v_lshrrev_b32 v43, 5, v2\n"
      "v_lshlrev_b32 v44, 4, v2\n"
      "s_lshl_b32 s52, s45, 15\n"
      "s_add_u32 s52, s52, s33\n"
      "v_add_u32 v13, s52, v44\n"
      "s_lshl_b32 s52, s38, 1\n"
      "s_add_u32 s52, s52, s45\n"
      "s_lshl_b32 s53, s52, 11\n"
      "s_add_u32 s53, s53, s33\n"
      "v_add_u32 v14, s53, v44\n"
      "s_lshl_b32 s52, s52, 6\n"
      "v_lshl_add_u32 v31, v43, 4, s52\n"
      "v_lshl_add_u32 v15, v42, 11, v31\n"
      "v_add_u32 v15, s33, v15\n"
      "v_and_b32 v31, 3, v40\n"
      "v_lshlrev_b32 v31, 7, v31\n"
      "v_add_u32 v31, v31, v41\n"
      "v_lshlrev_b32 v31, 4, v31\n"
      "s_lshl_b32 s51, s37, 2\n"
      "s_add_u32 s51, s51, s36\n"
      "s_lshl_b32 s51, s51, 15\n"
      "s_lshl_b32 s54, s38, 7\n"
      "s_add_u32 s51, s51, s54\n"
      "v_add_u32 v18, s51, v39\n"
      "v_xor_b32 v46, 64, v18\n"
      "v_add_u32 v19, 0x3c00, v46\n"
      "v_bfe_u32 v45, v42, 1, 3\n"
      "v_xor_b32 v45, v45, v43\n"
      "v_lshlrev_b32 v45, 4, v45\n"
      "v_lshl_add_u32 v45, v42, 7, v45\n"
      "v_add_u32 v9, s34, v45\n"
      "v_xor_b32 v10, 32, v9\n"
      "v_xor_b32 v11, 64, v9\n"
      "v_xor_b32 v12, 96, v9\n"
      "s_lshl_b32 s51, s45, 6\n"
      "v_xor_b32 v45, s51, v45\n"
      "s_lshl_b32 s51, s44, 12\n"
      "s_add_u32 s51, s51, 147456\n"
      "v_add_u32 v20, s51, v45\n"
      "v_xor_b32 v21, 32, v20\n"
      "v_bfe_u32 v45, v2, 2, 2\n"
      "v_lshl_add_u32 v46, v43, 3, v45\n"
      "v_lshlrev_b32 v46, 7, v46\n"
      "v_bfe_u32 v47, v2, 1, 1\n"
      "v_lshl_add_u32 v46, v47, 3, v46\n"
      "s_lshl_b32 s51, s45, 16\n"
      "v_add_u32 v46, s51, v46\n"
      "v_bfe_u32 v47, v2, 4, 1\n"
      "v_and_b32 v45, 2, v45\n"
      "v_xor_b32 v47, v47, v45\n"
      "s_lshl_b32 s51, s45, 1\n"
      "v_xor_b32 v47, s51, v47\n"
      "v_and_b32 v45, 1, v2\n"
      "v_lshl_add_u32 v47, v47, 1, v45\n"
      "v_lshl_add_u32 v3, v47, 4, v46\n"
      "v_xor_b32 v8, 64, v3\n"
      "s_lshl_b32 s51, s36, 12\n"
      "s_add_u32 s51, s51, 131072\n"
      "v_add_u32 v16, s51, v44\n"
      "v_xor_b32 v17, 0x2000, v16\n"
      "s_lshl_b32 s51, s37, 8\n"
      "s_add_u32 s51, s51, 0x400000\n"
      "v_lshl_add_u32 v24, v38, 2, s51\n"
      "s_lshl_b32 s51, s37, 12\n"
      "s_lshl_b32 s52, s38, 2\n"
      "s_add_u32 s51, s51, s52\n"
      "s_add_u32 s51, s51, 0x410000\n"
      "v_lshl_add_u32 v26, v38, 8, s51\n"
      "v_mov_b32 v252, -1\n"
      "v_mov_b32 v253, -1\n"
      "v_mov_b32 v254, -1\n"
      "v_mov_b32 v255, -1\n"
      "s_mov_b32 s26, 0x1800\n"
      "s_mov_b32 s27, 0x20000\n"
      "s_mov_b32 s30, 0x200000\n"
      "s_mov_b32 s31, 0x20000\n"
      "s_mov_b32 s62, 0x200000\n"
      "s_mov_b32 s46, 524288\n"
      "s_add_u32 m0, s39, 6144\n"
      "s_add_u32 s40, s40, 0x8000\n"
      "s_and_b32 s40, s40, 0xfffff\n"
      "buffer_load_dwordx4 v1, s[20:23], s40 offen  lds\n"
      "s_add_u32 m0, s34, 4096\n"
      "s_nop 0\n"
      "buffer_load_dwordx4 v7, s[16:19], s42 offen offset:3072  lds\n"
      "s_cmp_lg_u32 s36, 0\n"
      "s_cbranch_scc1 L_h1\n"
      "s_waitcnt vmcnt(9)\n"
      "v_add_u32 v27, 8, v2\n"
      "v_bfe_u32 v29, v29, v27, 1\n"
      "v_cmp_eq_u32 vcc, 0, v29\n"
      "s_nop 3\n"
      "s_and_b32 s47, vcc_lo, 0x1ffff\n"
      "s_lshl_b32 s47, s47, 8\n"
      "v_mov_b32 v29, s47\n"
      "s_mov_b64 exec, 1\n"
      "buffer_store_dword v29, v28, s[64:67], 0 offen sc1\n"
      "s_mov_b64 exec, -1\n"
      "L_h1:\n"
      "v_bfe_u32 v45, v2, 2, 3\n"
      "v_lshrrev_b32 v2, 5, v2\n"
      "v_lshl_add_u32 v2, v2, 4, v45\n"
      "v_lshlrev_b32 v2, 7, v2\n"
      "s_lshl_b32 s51, s59, 5\n"
      "s_add_u32 s51, s51, s60\n"
      "s_lshl_b32 s51, s51, 2\n"
      "s_add_u32 s51, s51, s36\n"
      "s_lshl_b32 s51, s51, 12\n"
      "s_and_b32 s52, s38, 8\n"
      "s_lshl_b32 s52, s52, 7\n"
      "s_add_u32 s58, s51, s52\n"
      "s_mov_b64 exec, 1\n"
      "buffer_load_dword v0, v15, s[28:31], 0 offen\n"
      "s_mov_b64 exec, -1\n"
      "s_waitcnt vmcnt(13)\n"
      "s_waitcnt vmcnt(12)\n"
      "s_waitcnt vmcnt(11)\n"
      "s_add_u32 m0, s39, 8192\n"
      "s_add_u32 s40, s40, 0x8000\n"
      "s_and_b32 s40, s40, 0xfffff\n"
      "buffer_load_dwordx4 v1, s[20:23], s40 offen  lds\n"
      "s_waitcnt vmcnt(11)\n"
      "s_add_u32 m0, s34, 8192\n"
      "s_add_u32 s42, s42, 128\n"
      "s_and_b32 s42, s42, 0x3ff\n"
      "buffer_load_dwordx4 v4, s[16:19], s42 offen offset:0  lds\n"
      "s_waitcnt vmcnt(10)\n"
      "s_add_u32 m0, s39, 10240\n"
      "s_add_u32 s40, s40, 0x8000\n"
      "s_and_b32 s40, s40, 0xfffff\n"
      "buffer_load_dwordx4 v1, s[20:23], s40 offen  lds\n"
      "s_waitcnt vmcnt(10)\n"
      "s_add_u32 m0, s34, 8192\n"
      "s_nop 0\n"
      "buffer_load_dwordx4 v5, s[16:19], s42 offen offset:1024  lds\n"
      "s_waitcnt vmcnt(10)\n"
      "s_add_u32 m0, s39, 12288\n"
      "s_add_u32 s40, s40, 0x8000\n"
      "s_and_b32 s40, s40, 0xfffff\n"
      "buffer_load_dwordx4 v1, s[20:23], s40 offen  lds\n"
      "s_waitcnt vmcnt(10)\n"
      "s_add_u32 m0, s34, 8192\n"
      "s_nop 0\n"
      "buffer_load_dwordx4 v6, s[16:19], s42 offen offset:2048  lds\n"
      "s_waitcnt vmcnt(10)\n"
      "s_add_u32 m0, s39, 14336\n"
      "s_add_u32 s40, s40, 0x8000\n"
      "s_and_b32 s40, s40, 0xfffff\n"
      "buffer_load_dwordx4 v1, s[20:23], s40 offen  lds\n"
      "s_waitcnt vmcnt(10)\n"
      "s_add_u32 m0, s34, 8192\n"
      "s_nop 0\n"
      "buffer_load_dwordx4 v7, s[16:19], s42 offen offset:3072  lds\n"
      "s_waitcnt vmcnt(10)\n"
      "s_add_u32 m0, s39, 16384\n"
      "s_add_u32 s40, s40, 0x8000\n"
      "s_and_b32 s40, s40, 0xfffff\n"
      "buffer_load_dwordx4 v1, s[20:23], s40 offen  lds\n"
      "s_barrier\n"
      "ds_read_b64_tr_b16 a[4:5], v8 offset:0\n"
      "ds_read_b64_tr_b16 a[6:7], v8 offset:512\n"
      "ds_read_b64_tr_b16 a[0:1], v3 offset:0\n"
      "ds_read_b64_tr_b16 a[2:3], v3 offset:512\n"
      "ds_read_b128 v[128:131], v9 offset:0\n"
      "ds_read_b64_tr_b16 a[12:13], v8 offset:2048\n"
      "ds_read_b64_tr_b16 a[14:15], v8 offset:2560\n"
      "ds_read_b64_tr_b16 a[8:9], v3 offset:2048\n"
      "ds_read_b64_tr_b16 a[10:11], v3 offset:2560\n"
      "ds_read_b128 v[132:135], v10 offset:0\n"
      "s_waitcnt lgkmcnt(5)\n"
      "ds_read_b64_tr_b16 a[20:21], v8 offset:4096\n"
      "ds_read_b64_tr_b16 a[22:23], v8 offset:4608\n"
      "ds_read_b64_tr_b16 a[16:17], v3 offset:4096\n"
      "ds_read_b64_tr_b16 a[18:19], v3 offset:4608\n"
      "ds_read_b128 v[136:139], v11 offset:0\n"
      "v_mfma_f32_32x32x16_bf16 v[80:95], a[4:7], v[128:131], 0\n"
      "s_waitcnt vmcnt(10)\n"
      "s_add_u32 m0, s34, 12288\n"
      "s_add_u32 s42, s42, 128\n"
      "s_and_b32 s42, s42, 0x3ff\n"
      "buffer_load_dwordx4 v4, s[16:19], s42 offen offset:0  lds\n"
      "v_mfma_f32_32x32x16_bf16 v[64:79], a[0:3], v[128:131], 0\n"
      "s_waitcnt vmcnt(9)\n"
      "s_add_u32 m0, s39, 18432\n"
      "s_add_u32 s40, s40, 0x8000\n"
      "s_and_b32 s40, s40, 0xfffff\n"
      "buffer_load_dwordx4 v1, s[20:23], s40 offen  lds\n"
      "s_waitcnt lgkmcnt(5)\n"
      "ds_read_b64_tr_b16 a[28:29], v8 offset:6144\n"
      "ds_read_b64_tr_b16 a[30:31], v8 offset:6656\n"
      "ds_read_b64_tr_b16 a[24:25], v3 offset:6144\n"
      "ds_read_b64_tr_b16 a[26:27], v3 offset:6656\n"
      "ds_read_b128 v[140:143], v12 offset:0\n"
      "v_mfma_f32_32x32x16_bf16 v[80:95], a[12:15], v[132:135], v[80:95]\n"
      "s_waitcnt vmcnt(9)\n"
      "s_add_u32 m0, s34, 12288\n"
      "s_nop 0\n"
      "buffer_load_dwordx4 v5, s[16:19], s42 offen offset:1024  lds\n"
      "v_mfma_f32_32x32x16_bf16 v[64:79], a[8:11], v[132:135], v[64:79]\n"
      "s_waitcnt vmcnt(9)\n"
      "s_add_u32 m0, s39, 20480\n"
      "s_add_u32 s40, s40, 0x8000\n"
      "s_and_b32 s40, s40, 0xfffff\n"
      "buffer_load_dwordx4 v1, s[20:23], s40 offen  lds\n"
      "s_waitcnt lgkmcnt(5)\n"
      "v_mfma_f32_32x32x16_bf16 v[80:95], a[20:23], v[136:139], v[80:95]\n"
      "s_waitcnt vmcnt(9)\n"
      "s_add_u32 m0, s34, 12288\n"
      "s_nop 0\n"
      "buffer_load_dwordx4 v6, s[16:19], s42 offen offset:2048  lds\n"
      "v_mfma_f32_32x32x16_bf16 v[64:79], a[16:19], v[136:139], v[64:79]\n"
      "s_waitcnt vmcnt(9)\n"
      "s_add_u32 m0, s39, 22528\n"
      "s_add_u32 s40, s40, 0x8000\n"
      "s_and_b32 s40, s40, 0xfffff\n"
      "buffer_load_dwordx4 v1, s[20:23], s40 offen  lds\n"
      "s_waitcnt lgkmcnt(0)\n"
      "v_mfma_f32_32x32x16_bf16 v[80:95], a[28:31], v[140:143], v[80:95]\n"
      "s_waitcnt vmcnt(9)\n"
      "s_add_u32 m0, s34, 12288\n"
      "s_nop 0\n"
      "buffer_load_dwordx4 v7, s[16:19], s42 offen offset:3072  lds\n"
      "v_mfma_f32_32x32x16_bf16 v[64:79], a[24:27], v[140:143], v[64:79]\n"
      "s_waitcnt vmcnt(9)\n"
      "s_add_u32 m0, s39, 24576\n"
      "s_add_u32 s40, s40, 0x8000\n"
      "s_and_b32 s40, s40, 0xfffff\n"
      "buffer_load_dwordx4 v1, s[20:23], s40 offen  lds\n"
      "s_barrier\n"
      "ds_read_b64_tr_b16 a[36:37], v8 offset:8192\n"
      "ds_read_b64_tr_b16 a[38:39], v8 offset:8704\n"
      "ds_read_b64_tr_b16 a[32:33], v3 offset:8192\n"
      "ds_read_b64_tr_b16 a[34:35], v3 offset:8704\n"
      "ds_read_b128 v[144:147], v9 offset:4096\n"
      "ds_read_b64_tr_b16 a[44:45], v8 offset:10240\n"
      "ds_read_b64_tr_b16 a[46:47], v8 offset:10752\n"
      "ds_read_b64_tr_b16 a[40:41], v3 offset:10240\n"
      "ds_read_b64_tr_b16 a[42:43], v3 offset:10752\n"
      "ds_read_b128 v[148:151], v10 offset:4096\n"
      "s_waitcnt lgkmcnt(5)\n"
      "ds_read_b64_tr_b16 a[52:53], v8 offset:12288\n"
      "ds_read_b64_tr_b16 a[54:55], v8 offset:12800\n"
      "ds_read_b64_tr_b16 a[48:49], v3 offset:12288\n"
      "ds_read_b64_tr_b16 a[50:51], v3 offset:12800\n"
      "ds_read_b128 v[152:155], v11 offset:4096\n"
      "v_mfma_f32_32x32x16_bf16 v[80:95], a[36:39], v[144:147], v[80:95]\n"
      "s_waitcnt vmcnt(9)\n"
      "s_add_u32 m0, s34, 0\n"
      "s_add_u32 s42, s42, 128\n"
      "s_and_b32 s42, s42, 0x3ff\n"
      "buffer_load_dwordx4 v4, s[16:19], s42 offen offset:0  lds\n"
      "v_mfma_f32_32x32x16_bf16 v[64:79], a[32:35], v[144:147], v[64:79]\n"
      "s_waitcnt vmcnt(9)\n"
      "s_add_u32 m0, s39, 26624\n"
      "s_add_u32 s40, s40, 0x8000\n"
      "s_and_b32 s40, s40, 0xfffff\n"
      "buffer_load_dwordx4 v1, s[20:23], s40 offen  lds\n"
      "buffer_store_dwordx4 v[252:255], v14, s[64:67], 0 offen offset:0 sc1\n"
      "buffer_store_dwordx4 v[252:255], v14, s[64:67], 0 offen offset:1024 sc1\n"
      "s_waitcnt lgkmcnt(5)\n"
      "ds_read_b64_tr_b16 a[60:61], v8 offset:14336\n"
      "ds_read_b64_tr_b16 a[62:63], v8 offset:14848\n"
      "ds_read_b64_tr_b16 a[56:57], v3 offset:14336\n"
      "ds_read_b64_tr_b16 a[58:59], v3 offset:14848\n"
      "ds_read_b128 v[156:159], v12 offset:4096\n"
      "v_mfma_f32_32x32x16_bf16 v[80:95], a[44:47], v[148:151], v[80:95]\n"
      "s_waitcnt vmcnt(11)\n"
      "s_add_u32 m0, s34, 0\n"
      "s_nop 0\n"
      "buffer_load_dwordx4 v5, s[16:19], s42 offen offset:1024  lds\n"
      "v_mfma_f32_32x32x16_bf16 v[64:79], a[40:43], v[148:151], v[64:79]\n"
      "s_waitcnt vmcnt(11)\n"
      "s_add_u32 m0, s39, 28672\n"
      "s_add_u32 s40, s40, 0x8000\n"
      "s_and_b32 s40, s40, 0xfffff\n"
      "buffer_load_dwordx4 v1, s[20:23], s40 offen  lds\n"
      "s_waitcnt lgkmcnt(5)\n"
      "v_mfma_f32_32x32x16_bf16 v[80:95], a[52:55], v[152:155], v[80:95]\n"
      "s_waitcnt vmcnt(11)\n"
      "s_add_u32 m0, s34, 0\n"
      "s_nop 0\n"
      "buffer_load_dwordx4 v6, s[16:19], s42 offen offset:2048  lds\n"
      "v_mfma_f32_32x32x16_bf16 v[64:79], a[48:51], v[152:155], v[64:79]\n"
      "s_waitcnt vmcnt(11)\n"
      "s_add_u32 m0, s39, 30720\n"
      "s_add_u32 s40, s40, 0x8000\n"
      "s_and_b32 s40, s40, 0xfffff\n"
      "buffer_load_dwordx4 v1, s[20:23], s40 offen  lds\n"
      "s_waitcnt lgkmcnt(0)\n"
      "v_mfma_f32_32x32x16_bf16 v[80:95], a[60:63], v[156:159], v[80:95]\n"
      "s_waitcnt vmcnt(11)\n"
      "s_add_u32 m0, s34, 0\n"
      "s_nop 0\n"
      "buffer_load_dwordx4 v7, s[16:19], s42 offen offset:3072  lds\n"
      "v_mfma_f32_32x32x16_bf16 v[64:79], a[56:59], v[156:159], v[64:79]\n"
      "s_waitcnt vmcnt(11)\n"
      "s_add_u32 m0, s39, 0\n"
      "s_add_u32 s40, s40, 0x8000\n"
      "s_and_b32 s40, s40, 0xfffff\n"
      "buffer_load_dwordx4 v1, s[20:23], s40 offen  lds\n"
      "s_cmp_lg_u32 s36, 0\n"
      "s_cbranch_scc1 L_h2\n"
      "buffer_load_dword v28, v24, s[64:67], 0 offen sc1\n"
      "L_h2:\n"
      "s_barrier\n"
      "ds_read_b64_tr_b16 a[68:69], v8 offset:16384\n"
      "ds_read_b64_tr_b16 a[70:71], v8 offset:16896\n"
      "ds_read_b64_tr_b16 a[64:65], v3 offset:16384\n"
      "ds_read_b64_tr_b16 a[66:67], v3 offset:16896\n"
      "ds_read_b128 v[160:163], v9 offset:8192\n"
      "ds_read_b64_tr_b16 a[76:77], v8 offset:18432\n"
      "ds_read_b64_tr_b16 a[78:79], v8 offset:18944\n"
      "ds_read_b64_tr_b16 a[72:73], v3 offset:18432\n"
      "ds_read_b64_tr_b16 a[74:75], v3 offset:18944\n"
      "ds_read_b128 v[164:167], v10 offset:8192\n"
      "s_waitcnt lgkmcnt(5)\n"
      "ds_read_b64_tr_b16 a[84:85], v8 offset:20480\n"
      "ds_read_b64_tr_b16 a[86:87], v8 offset:20992\n"
      "ds_read_b64_tr_b16 a[80:81], v3 offset:20480\n"
      "ds_read_b64_tr_b16 a[82:83], v3 offset:20992\n"
      "ds_read_b128 v[168:171], v11 offset:8192\n"
      "v_mfma_f32_32x32x16_bf16 v[80:95], a[68:71], v[160:163], v[80:95]\n"
      "s_waitcnt vmcnt(11)\n"
      "s_add_u32 m0, s34, 4096\n"
      "s_add_u32 s42, s42, 128\n"
      "s_and_b32 s42, s42, 0x3ff\n"
      "buffer_load_dwordx4 v4, s[16:19], s42 offen offset:0  lds\n"
      "v_mfma_f32_32x32x16_bf16 v[64:79], a[64:67], v[160:163], v[64:79]\n"
      "s_waitcnt vmcnt(11)\n"
      "s_add_u32 m0, s39, 2048\n"
      "s_add_u32 s40, s40, 0x8000\n"
      "s_and_b32 s40, s40, 0xfffff\n"
      "buffer_load_dwordx4 v1, s[20:23], s40 offen  lds\n"
      "s_lshl_b32 s51, s36, 11\n"
      "s_add_u32 m0, s51, 147456\n"
      "s_nop 0\n"
      "s_cmp_lg_u32 s36, 3\n"
      "s_cbranch_scc1 L_lb\n"
      "s_mov_b32 s52, m0\n"
      "s_mov_b32 m0, 155648\n"
      "s_lshl_b32 s51, s38, 7\n"
      "buffer_load_dwordx4 v31, s[24:27], s51 offen lds\n"
      "s_mov_b32 m0, s52\n"
      "L_lb:\n"
      "s_lshl_b32 s51, s45, 6\n"
      "s_add_u32 s51, s51, 155648\n"
      "v_mbcnt_lo_u32_b32 v31, -1, 0\n"
      "v_and_b32 v31, 32, v31\n"
      "v_lshrrev_b32 v31, 1, v31\n"
      "v_add_u32 v31, s51, v31\n"
      "buffer_load_dwordx4 v18, s[16:19], 0 offen offset:0 lds\n"
      "buffer_load_dwordx4 v19, s[16:19], 0 offen offset:1024 lds\n"
      "s_waitcnt lgkmcnt(5)\n"
      "ds_read_b64_tr_b16 a[92:93], v8 offset:22528\n"
      "ds_read_b64_tr_b16 a[94:95], v8 offset:23040\n"
      "ds_read_b64_tr_b16 a[88:89], v3 offset:22528\n"
      "ds_read_b64_tr_b16 a[90:91], v3 offset:23040\n"
      "ds_read_b128 v[172:175], v12 offset:8192\n"
      "v_mfma_f32_32x32x16_bf16 v[80:95], a[76:79], v[164:167], v[80:95]\n"
      "s_waitcnt vmcnt(13)\n"
      "s_add_u32 m0, s34, 4096\n"
      "s_nop 0\n"
      "buffer_load_dwordx4 v5, s[16:19], s42 offen offset:1024  lds\n"
      "v_mfma_f32_32x32x16_bf16 v[64:79], a[72:75], v[164:167], v[64:79]\n"
      "s_waitcnt vmcnt(13)\n"
      "s_add_u32 m0, s39, 4096\n"
      "s_add_u32 s40, s40, 0x8000\n"
      "s_and_b32 s40, s40, 0xfffff\n"
      "buffer_load_dwordx4 v1, s[20:23], s40 offen  lds\n"
      "s_waitcnt lgkmcnt(5)\n"
      "v_mfma_f32_32x32x16_bf16 v[80:95], a[84:87], v[168:171], v[80:95]\n"
      "s_waitcnt vmcnt(11)\n"
      "s_add_u32 m0, s34, 4096\n"
      "s_nop 0\n"
      "buffer_load_dwordx4 v6, s[16:19], s42 offen offset:2048  lds\n"
      "v_mfma_f32_32x32x16_bf16 v[64:79], a[80:83], v[168:171], v[64:79]\n"
      "s_waitcnt vmcnt(11)\n"
      "s_add_u32 m0, s39, 6144\n"
      "s_add_u32 s40, s40, 0x8000\n"
      "s_and_b32 s40, s40, 0xfffff\n"
      "buffer_load_dwordx4 v1, s[20:23], s40 offen  lds\n"
      "s_waitcnt lgkmcnt(0)\n"
      "v_mfma_f32_32x32x16_bf16 v[80:95], a[92:95], v[172:175], v[80:95]\n"
      "s_waitcnt vmcnt(11)\n"
      "s_add_u32 m0, s34, 4096\n"
      "s_nop 0\n"
      "buffer_load_dwordx4 v7, s[16:19], s42 offen offset:3072  lds\n"
      "v_mfma_f32_32x32x16_bf16 v[64:79], a[88:91], v[172:175], v[64:79]\n"
      "s_waitcnt vmcnt(11)\n"
      "s_add_u32 m0, s39, 8192\n"
      "s_add_u32 s40, s40, 0x8000\n"
      "s_and_b32 s40, s40, 0xfffff\n"
      "buffer_load_dwordx4 v1, s[20:23], s40 offen  lds\n"
      "s_add_u32 s51, s58, 0x200000\n"
      "buffer_load_dword v0, v2, s[20:23], s51 offen\n"
      "s_waitcnt vmcnt(17)\n"
      "s_waitcnt vmcnt(7)\n"
      "s_barrier\n"
      "ds_read_b64_tr_b16 a[100:101], v8 offset:24576\n"
      "ds_read_b64_tr_b16 a[102:103], v8 offset:25088\n"
      "ds_read_b64_tr_b16 a[96:97], v3 offset:24576\n"
      "ds_read_b64_tr_b16 a[98:99], v3 offset:25088\n"
      "ds_read_b128 v[176:179], v9 offset:12288\n"
      "ds_read_b64_tr_b16 a[108:109], v8 offset:26624\n"
      "ds_read_b64_tr_b16 a[110:111], v8 offset:27136\n"
      "ds_read_b64_tr_b16 a[104:105], v3 offset:26624\n"
      "ds_read_b64_tr_b16 a[106:107], v3 offset:27136\n"
      "ds_read_b128 v[180:183], v10 offset:12288\n"
      "s_waitcnt lgkmcnt(5)\n"
      "ds_read_b64_tr_b16 a[116:117], v8 offset:28672\n"
      "ds_read_b64_tr_b16 a[118:119], v8 offset:29184\n"
      "ds_read_b64_tr_b16 a[112:113], v3 offset:28672\n"
      "ds_read_b64_tr_b16 a[114:115], v3 offset:29184\n"
      "ds_read_b128 v[184:187], v11 offset:12288\n"
      "v_mfma_f32_32x32x16_bf16 v[80:95], a[100:103], v[176:179], v[80:95]\n"
      "s_waitcnt vmcnt(12)\n"
      "s_add_u32 m0, s34, 8192\n"
      "s_add_u32 s42, s42, 128\n"
      "s_and_b32 s42, s42, 0x3ff\n"
      "buffer_load_dwordx4 v4, s[16:19], s42 offen offset:0  lds\n"
      "v_mfma_f32_32x32x16_bf16 v[64:79], a[96:99], v[176:179], v[64:79]\n"
      "s_waitcnt vmcnt(12)\n"
      "s_add_u32 m0, s39, 10240\n"
      "s_add_u32 s40, s40, 0x8000\n"
      "s_and_b32 s40, s40, 0xfffff\n"
      "buffer_load_dwordx4 v1, s[20:23], s40 offen  lds\n"
      "s_cmp_lg_u32 s36, 0\n"
      "s_cbranch_scc1 L_h3\n"
      "s_waitcnt vmcnt(13)\n"
      "s_mov_b64 exec, 0xffff\n"
      "v_and_b32 v29, 0xffffff00, v28\n"
      "v_or_b32 v29, 1, v29\n"
      "buffer_store_dword v29, v26, s[64:67], 0 offen sc1\n"
      "s_mov_b64 exec, -1\n"
      "L_h3:\n"
      "ds_read_b128 v[96:99], v31 offset:0\n"
      "ds_read_b128 v[100:103], v31 offset:32\n"
      "s_waitcnt lgkmcnt(8)\n"
      "ds_read_b128 v[104:107], v31 offset:128\n"
      "ds_read_b128 v[108:111], v31 offset:160\n"
      "ds_read_b128 v[112:115], v31 offset:256\n"
      "ds_read_b128 v[116:119], v31 offset:288\n"
      "s_waitcnt lgkmcnt(8)\n"
      "ds_read_b128 v[120:123], v20\n"
      "ds_read_b128 v[124:127], v21\n"
      "ds_read_b64_tr_b16 a[124:125], v8 offset:30720\n"
      "ds_read_b64_tr_b16 a[126:127], v8 offset:31232\n"
      "s_waitcnt lgkmcnt(8)\n"
      "ds_read_b64_tr_b16 a[120:121], v3 offset:30720\n"
      "ds_read_b64_tr_b16 a[122:123], v3 offset:31232\n"
      "ds_read_b128 v[188:191], v12 offset:12288\n"
      "v_mfma_f32_32x32x16_bf16 v[80:95], a[108:111], v[180:183], v[80:95]\n"
      "s_waitcnt vmcnt(12)\n"
      "s_add_u32 m0, s34, 8192\n"
      "s_nop 0\n"
      "buffer_load_dwordx4 v5, s[16:19], s42 offen offset:1024  lds\n"
      "v_mfma_f32_32x32x16_bf16 v[64:79], a[104:107], v[180:183], v[64:79]\n"
      "s_waitcnt vmcnt(12)\n"
      "s_add_u32 m0, s39, 12288\n"
      "s_add_u32 s40, s40, 0x8000\n"
      "s_and_b32 s40, s40, 0xfffff\n"
      "buffer_load_dwordx4 v1, s[20:23], s40 offen  lds\n"
      "s_waitcnt lgkmcnt(5)\n"
      "v_mfma_f32_32x32x16_bf16 v[80:95], a[116:119], v[184:187], v[80:95]\n"
      "v_lshlrev_b32 v32, 16, v120\n"
      "v_and_b32 v33, 0xffff0000, v120\n"
      "v_lshlrev_b32 v34, 16, v121\n"
      "v_and_b32 v35, 0xffff0000, v121\n"
      "s_waitcnt vmcnt(10)\n"
      "s_add_u32 m0, s34, 8192\n"
      "s_nop 0\n"
      "buffer_load_dwordx4 v6, s[16:19], s42 offen offset:2048  lds\n"
      "v_mfma_f32_32x32x16_bf16 v[64:79], a[112:115], v[184:187], v[64:79]\n"
      "v_lshlrev_b32 v36, 16, v122\n"
      "v_and_b32 v37, 0xffff0000, v122\n"
      "v_lshlrev_b32 v38, 16, v123\n"
      "v_and_b32 v39, 0xffff0000, v123\n"
      "s_waitcnt vmcnt(10)\n"
      "s_add_u32 m0, s39, 14336\n"
      "s_add_u32 s40, s40, 0x8000\n"
      "s_and_b32 s40, s40, 0xfffff\n"
      "buffer_load_dwordx4 v1, s[20:23], s40 offen  lds\n"
      "s_waitcnt lgkmcnt(0)\n"
      "v_mfma_f32_32x32x16_bf16 v[80:95], a[124:127], v[188:191], v[80:95]\n"
      "v_lshlrev_b32 v40, 16, v124\n"
      "v_and_b32 v41, 0xffff0000, v124\n"
      "v_lshlrev_b32 v42, 16, v125\n"
      "v_and_b32 v43, 0xffff0000, v125\n"
      "s_waitcnt vmcnt(10)\n"
      "s_add_u32 m0, s34, 8192\n"
      "s_nop 0\n"
      "buffer_load_dwordx4 v7, s[16:19], s42 offen offset:3072  lds\n"
      "v_mfma_f32_32x32x16_bf16 v[64:79], a[120:123], v[188:191], v[64:79]\n"
      "v_lshlrev_b32 v44, 16, v126\n"
      "v_and_b32 v45, 0xffff0000, v126\n"
      "v_lshlrev_b32 v46, 16, v127\n"
      "v_and_b32 v47, 0xffff0000, v127\n"
      "s_waitcnt vmcnt(10)\n"
      "s_add_u32 m0, s39, 16384\n"
      "s_add_u32 s40, s40, 0x8000\n"
      "s_and_b32 s40, s40, 0xfffff\n"
      "buffer_load_dwordx4 v1, s[20:23], s40 offen  lds\n"
      "s_barrier\n"
      "ds_read_b64_tr_b16 a[132:133], v8 offset:0\n"
      "ds_read_b64_tr_b16 a[134:135], v8 offset:512\n"
      "ds_read_b64_tr_b16 a[128:129], v3 offset:0\n"
      "ds_read_b64_tr_b16 a[130:131], v3 offset:512\n"
      "ds_read_b128 v[192:195], v9 offset:0\n"
      "ds_read_b64_tr_b16 a[140:141], v8 offset:2048\n"
      "ds_read_b64_tr_b16 a[142:143], v8 offset:2560\n"
      "ds_read_b64_tr_b16 a[136:137], v3 offset:2048\n"
      "ds_read_b64_tr_b16 a[138:139], v3 offset:2560\n"
      "ds_read_b128 v[196:199], v10 offset:0\n"
      "s_waitcnt lgkmcnt(5)\n"
      "ds_read_b64_tr_b16 a[148:149], v8 offset:4096\n"
      "ds_read_b64_tr_b16 a[150:151], v8 offset:4608\n"
      "ds_read_b64_tr_b16 a[144:145], v3 offset:4096\n"
      "ds_read_b64_tr_b16 a[146:147], v3 offset:4608\n"
      "ds_read_b128 v[200:203], v11 offset:0\n"
      "v_mfma_f32_32x32x16_bf16 v[80:95], a[132:135], v[192:195], v[80:95]\n"
      "v_lshlrev_b32 v48, 16, v120\n"
      "v_and_b32 v49, 0xffff0000, v120\n"
      "v_lshlrev_b32 v128, 16, v96\n"
      "v_and_b32 v129, 0xffff0000, v96\n"
      "s_waitcnt vmcnt(10)\n"
      "s_add_u32 m0, s34, 12288\n"
      "s_add_u32 s42, s42, 128\n"
      "s_and_b32 s42, s42, 0x3ff\n"
      "buffer_load_dwordx4 v4, s[16:19], s42 offen offset:0  lds\n"
      "v_mfma_f32_32x32x16_bf16 v[64:79], a[128:131], v[192:195], v[64:79]\n"
      "v_pk_fma_f32 v[48:49], v[32:33], v[128:129], v[48:49]\n"
      "v_lshlrev_b32 v50, 16, v121\n"
      "v_and_b32 v51, 0xffff0000, v121\n"
      "v_lshlrev_b32 v130, 16, v97\n"
      "s_waitcnt vmcnt(10)\n"
      "s_add_u32 m0, s39, 18432\n"
      "s_add_u32 s40, s40, 0x8000\n"
      "s_and_b32 s40, s40, 0xfffff\n"
      "buffer_load_dwordx4 v1, s[20:23], s40 offen  lds\n"
      "s_waitcnt lgkmcnt(5)\n"
      "ds_read_b64_tr_b16 a[156:157], v8 offset:6144\n"
      "ds_read_b64_tr_b16 a[158:159], v8 offset:6656\n"
      "ds_read_b64_tr_b16 a[152:153], v3 offset:6144\n"
      "ds_read_b64_tr_b16 a[154:155], v3 offset:6656\n"
      "ds_read_b128 v[204:207], v12 offset:0\n"
      "v_mfma_f32_32x32x16_bf16 v[80:95], a[140:143], v[196:199], v[80:95]\n"
      "v_and_b32 v131, 0xffff0000, v97\n"
      "v_pk_fma_f32 v[50:51], v[34:35], v[130:131], v[50:51]\n"
      "v_lshlrev_b32 v52, 16, v122\n"
      "v_and_b32 v53, 0xffff0000, v122\n"
      "s_waitcnt vmcnt(9)\n"
      "s_add_u32 m0, s34, 12288\n"
      "s_nop 0\n"
      "buffer_load_dwordx4 v5, s[16:19], s42 offen offset:1024  lds\n"
      "v_mfma_f32_32x32x16_bf16 v[64:79], a[136:139], v[196:199], v[64:79]\n"
      "v_lshlrev_b32 v132, 16, v98\n"
      "v_and_b32 v133, 0xffff0000, v98\n"
      "v_pk_fma_f32 v[52:53], v[36:37], v[132:133], v[52:53]\n"
      "v_lshlrev_b32 v54, 16, v123\n"
      "s_waitcnt vmcnt(9)\n"
      "s_add_u32 m0, s39, 20480\n"
      "s_add_u32 s40, s40, 0x8000\n"
      "s_and_b32 s40, s40, 0xfffff\n"
      "buffer_load_dwordx4 v1, s[20:23], s40 offen  lds\n"
      "s_waitcnt lgkmcnt(5)\n"
      "v_mfma_f32_32x32x16_bf16 v[80:95], a[148:151], v[200:203], v[80:95]\n"
      "v_and_b32 v55, 0xffff0000, v123\n"
      "v_lshlrev_b32 v134, 16, v99\n"
      "v_and_b32 v135, 0xffff0000, v99\n"
      "v_pk_fma_f32 v[54:55], v[38:39], v[134:135], v[54:55]\n"
      "s_waitcnt vmcnt(9)\n"
      "s_add_u32 m0, s34, 12288\n"
      "s_nop 0\n"
      "buffer_load_dwordx4 v6, s[16:19], s42 offen offset:2048  lds\n"
      "v_mfma_f32_32x32x16_bf16 v[64:79], a[144:147], v[200:203], v[64:79]\n"
      "v_lshlrev_b32 v56, 16, v124\n"
      "v_and_b32 v57, 0xffff0000, v124\n"
      "v_lshlrev_b32 v136, 16, v100\n"
      "v_and_b32 v137, 0xffff0000, v100\n"
      "s_waitcnt vmcnt(9)\n"
      "s_add_u32 m0, s39, 22528\n"
      "s_add_u32 s40, s40, 0x8000\n"
      "s_and_b32 s40, s40, 0xfffff\n"
      "buffer_load_dwordx4 v1, s[20:23], s40 offen  lds\n"
      "s_waitcnt lgkmcnt(0)\n"
      "v_mfma_f32_32x32x16_bf16 v[80:95], a[156:159], v[204:207], v[80:95]\n"
      "v_pk_fma_f32 v[56:57], v[40:41], v[136:137], v[56:57]\n"
      "v_lshlrev_b32 v58, 16, v125\n"
      "v_and_b32 v59, 0xffff0000, v125\n"
      "v_lshlrev_b32 v138, 16, v101\n"
      "s_waitcnt vmcnt(9)\n"
      "s_add_u32 m0, s34, 12288\n"
      "s_nop 0\n"
      "buffer_load_dwordx4 v7, s[16:19], s42 offen offset:3072  lds\n"
      "v_mfma_f32_32x32x16_bf16 v[64:79], a[152:155], v[204:207], v[64:79]\n"
      "v_and_b32 v139, 0xffff0000, v101\n"
      "v_pk_fma_f32 v[58:59], v[42:43], v[138:139], v[58:59]\n"
      "v_lshlrev_b32 v60, 16, v126\n"
      "v_and_b32 v61, 0xffff0000, v126\n"
      "s_waitcnt vmcnt(9)\n"
      "s_add_u32 m0, s39, 24576\n"
      "s_add_u32 s40, s40, 0x8000\n"
      "s_and_b32 s40, s40, 0xfffff\n"
      "buffer_load_dwordx4 v1, s[20:23], s40 offen  lds\n"
      "s_barrier\n"
      "ds_read_b64_tr_b16 a[164:165], v8 offset:8192\n"
      "ds_read_b64_tr_b16 a[166:167], v8 offset:8704\n"
      "ds_read_b64_tr_b16 a[160:161], v3 offset:8192\n"
      "ds_read_b64_tr_b16 a[162:163], v3 offset:8704\n"
      "ds_read_b128 v[208:211], v9 offset:4096\n"
      "ds_read_b64_tr_b16 a[172:173], v8 offset:10240\n"
      "ds_read_b64_tr_b16 a[174:175], v8 offset:10752\n"
      "ds_read_b64_tr_b16 a[168:169], v3 offset:10240\n"
      "ds_read_b64_tr_b16 a[170:171], v3 offset:10752\n"
      "ds_read_b128 v[212:215], v10 offset:4096\n"
      "s_waitcnt lgkmcnt(5)\n"
      "ds_read_b64_tr_b16 a[180:181], v8 offset:12288\n"
      "ds_read_b64_tr_b16 a[182:183], v8 offset:12800\n"
      "ds_read_b64_tr_b16 a[176:177], v3 offset:12288\n"
      "ds_read_b64_tr_b16 a[178:179], v3 offset:12800\n"
      "ds_read_b128 v[216:219], v11 offset:4096\n"
      "v_mfma_f32_32x32x16_bf16 v[80:95], a[164:167], v[208:211], v[80:95]\n"
      "v_lshlrev_b32 v140, 16, v102\n"
      "v_and_b32 v141, 0xffff0000, v102\n"
      "v_pk_fma_f32 v[60:61], v[44:45], v[140:141], v[60:61]\n"
      "v_lshlrev_b32 v62, 16, v127\n"
      "s_waitcnt vmcnt(9)\n"
      "s_add_u32 m0, s39, 26624\n"
      "s_add_u32 s40, s40, 0x8000\n"
      "s_and_b32 s40, s40, 0xfffff\n"
      "buffer_load_dwordx4 v1, s[20:23], s40 offen  lds\n"
      "v_mfma_f32_32x32x16_bf16 v[64:79], a[160:163], v[208:211], v[64:79]\n"
      "v_and_b32 v63, 0xffff0000, v127\n"
      "v_lshlrev_b32 v142, 16, v103\n"
      "v_and_b32 v143, 0xffff0000, v103\n"
      "v_pk_fma_f32 v[62:63], v[46:47], v[142:143], v[62:63]\n"
      "s_waitcnt vmcnt(9)\n"
      "s_add_u32 m0, s39, 28672\n"
      "s_add_u32 s40, s40, 0x8000\n"
      "s_and_b32 s40, s40, 0xfffff\n"
      "buffer_load_dwordx4 v1, s[20:23], s40 offen  lds\n"
      "s_waitcnt lgkmcnt(5)\n"
      "ds_read_b64_tr_b16 a[188:189], v8 offset:14336\n"
      "ds_read_b64_tr_b16 a[190:191], v8 offset:14848\n"
      "ds_read_b64_tr_b16 a[184:185], v3 offset:14336\n"
      "ds_read_b64_tr_b16 a[186:187], v3 offset:14848\n"
      "ds_read_b128 v[220:223], v12 offset:4096\n"
      "v_mfma_f32_32x32x16_bf16 v[80:95], a[172:175], v[212:215], v[80:95]\n"
      "s_waitcnt vmcnt(9)\n"
      "s_add_u32 m0, s39, 30720\n"
      "s_add_u32 s40, s40, 0x8000\n"
      "s_and_b32 s40, s40, 0xfffff\n"
      "buffer_load_dwordx4 v1, s[20:23], s40 offen  lds\n"
      "v_mfma_f32_32x32x16_bf16 v[64:79], a[168:171], v[212:215], v[64:79]\n"
      "s_waitcnt vmcnt(9)\n"
      "s_add_u32 m0, s39, 0\n"
      "s_lshl_b32 s40, s49, 15\n"
      "v_add_u32 v1, 0x200000, v1\n"
      "buffer_load_dwordx4 v1, s[20:23], s40 offen  lds\n"
      "s_waitcnt lgkmcnt(5)\n"
      "v_mfma_f32_32x32x16_bf16 v[80:95], a[180:183], v[216:219], v[80:95]\n"
      "s_waitcnt vmcnt(9)\n"
      "s_add_u32 m0, s39, 2048\n"
      "s_add_u32 s40, s40, 0x8000\n"
      "s_and_b32 s40, s40, 0xfffff\n"
      "buffer_load_dwordx4 v1, s[20:23], s40 offen  lds\n"
      "v_mfma_f32_32x32x16_bf16 v[64:79], a[176:179], v[216:219], v[64:79]\n"
      "s_waitcnt vmcnt(9)\n"
      "s_add_u32 m0, s39, 4096\n"
      "s_add_u32 s40, s40, 0x8000\n"
      "s_and_b32 s40, s40, 0xfffff\n"
      "buffer_load_dwordx4 v1, s[20:23], s40 offen  lds\n"
      "s_waitcnt lgkmcnt(0)\n"
      "v_mfma_f32_32x32x16_bf16 v[80:95], a[188:191], v[220:223], v[80:95]\n"
      "s_waitcnt vmcnt(9)\n"
      "s_add_u32 m0, s39, 6144\n"
      "s_add_u32 s40, s40, 0x8000\n"
      "s_and_b32 s40, s40, 0xfffff\n"
      "buffer_load_dwordx4 v1, s[20:23], s40 offen  lds\n"
      "v_mfma_f32_32x32x16_bf16 v[64:79], a[184:187], v[220:223], v[64:79]\n"
      "s_waitcnt vmcnt(9)\n"
      "s_barrier\n"
      "ds_read_b64_tr_b16 a[196:197], v8 offset:16384\n"
      "ds_read_b64_tr_b16 a[198:199], v8 offset:16896\n"
      "ds_read_b64_tr_b16 a[192:193], v3 offset:16384\n"
      "ds_read_b64_tr_b16 a[194:195], v3 offset:16896\n"
      "ds_read_b128 v[224:227], v9 offset:8192\n"
      "ds_read_b64_tr_b16 a[204:205], v8 offset:18432\n"
      "ds_read_b64_tr_b16 a[206:207], v8 offset:18944\n"
      "ds_read_b64_tr_b16 a[200:201], v3 offset:18432\n"
      "ds_read_b64_tr_b16 a[202:203], v3 offset:18944\n"
      "ds_read_b128 v[228:231], v10 offset:8192\n"
      "s_waitcnt lgkmcnt(5)\n"
      "ds_read_b64_tr_b16 a[212:213], v8 offset:20480\n"
      "ds_read_b64_tr_b16 a[214:215], v8 offset:20992\n"
      "ds_read_b64_tr_b16 a[208:209], v3 offset:20480\n"
      "ds_read_b64_tr_b16 a[210:211], v3 offset:20992\n"
      "ds_read_b128 v[232:235], v11 offset:8192\n"
      "v_mfma_f32_32x32x16_bf16 v[80:95], a[196:199], v[224:227], v[80:95]\n"
      "s_waitcnt vmcnt(8)\n"
      "s_add_u32 m0, s39, 8192\n"
      "s_add_u32 s40, s40, 0x8000\n"
      "s_and_b32 s40, s40, 0xfffff\n"
      "buffer_load_dwordx4 v1, s[20:23], s40 offen  lds\n"
      "s_add_u32 m0, s39, 10240\n"
      "s_add_u32 s40, s40, 0x8000\n"
      "s_and_b32 s40, s40, 0xfffff\n"
      "buffer_load_dwordx4 v1, s[20:23], s40 offen  lds\n"
      "v_mfma_f32_32x32x16_bf16 v[64:79], a[192:195], v[224:227], v[64:79]\n"
      "s_waitcnt vmcnt(9)\n"
      "s_add_u32 m0, s39, 12288\n"
      "s_add_u32 s40, s40, 0x8000\n"
      "s_and_b32 s40, s40, 0xfffff\n"
      "buffer_load_dwordx4 v1, s[20:23], s40 offen  lds\n"
      "s_waitcnt lgkmcnt(5)\n"
      "ds_read_b64_tr_b16 a[220:221], v8 offset:22528\n"
      "ds_read_b64_tr_b16 a[222:223], v8 offset:23040\n"
      "ds_read_b64_tr_b16 a[216:217], v3 offset:22528\n"
      "ds_read_b64_tr_b16 a[218:219], v3 offset:23040\n"
      "ds_read_b128 v[236:239], v12 offset:8192\n"
      "v_mfma_f32_32x32x16_bf16 v[80:95], a[204:207], v[228:231], v[80:95]\n"
      "s_waitcnt vmcnt(9)\n"
      "s_add_u32 m0, s39, 14336\n"
      "s_add_u32 s40, s40, 0x8000\n"
      "s_and_b32 s40, s40, 0xfffff\n"
      "buffer_load_dwordx4 v1, s[20:23], s40 offen  lds\n"
      "v_mfma_f32_32x32x16_bf16 v[64:79], a[200:203], v[228:231], v[64:79]\n"
      "s_cmp_lg_u32 s36, 0\n"
      "s_cbranch_scc1 L_h3b\n"
      "buffer_load_dword v29, v25, s[64:67], 0 offen sc1\n"
      "L_h3b:\n"
      "s_waitcnt lgkmcnt(5)\n"
      "v_mfma_f32_32x32x16_bf16 v[80:95], a[212:215], v[232:235], v[80:95]\n"
      "v_mfma_f32_32x32x16_bf16 v[64:79], a[208:211], v[232:235], v[64:79]\n"
      "s_waitcnt lgkmcnt(0)\n"
      "v_mfma_f32_32x32x16_bf16 v[80:95], a[220:223], v[236:239], v[80:95]\n"
      "v_mfma_f32_32x32x16_bf16 v[64:79], a[216:219], v[236:239], v[64:79]\n"
      "s_add_u32 s51, s63, 0x200000\n"
      "buffer_load_dword v0, v30, s[20:23], s51 offen\n"
      "s_barrier\n"
      "ds_read_b64_tr_b16 a[228:229], v8 offset:24576\n"
      "ds_read_b64_tr_b16 a[230:231], v8 offset:25088\n"
      "ds_read_b64_tr_b16 a[224:225], v3 offset:24576\n"
      "ds_read_b64_tr_b16 a[226:227], v3 offset:25088\n"
      "ds_read_b128 v[240:243], v9 offset:12288\n"
      "ds_read_b64_tr_b16 a[236:237], v8 offset:26624\n"
      "ds_read_b64_tr_b16 a[238:239], v8 offset:27136\n"
      "ds_read_b64_tr_b16 a[232:233], v3 offset:26624\n"
      "ds_read_b64_tr_b16 a[234:235], v3 offset:27136\n"
      "ds_read_b128 v[244:247], v10 offset:12288\n"
      "s_waitcnt lgkmcnt(5)\n"
      "v_mfma_f32_32x32x16_bf16 v[80:95], a[228:231], v[240:243], v[80:95]\n"
      "v_mfma_f32_32x32x16_bf16 v[64:79], a[224:227], v[240:243], v[64:79]\n"
      "s_waitcnt lgkmcnt(0)\n"
      "v_mfma_f32_32x32x16_bf16 v[80:95], a[236:239], v[244:247], v[80:95]\n"
      "v_mfma_f32_32x32x16_bf16 v[64:79], a[232:235], v[244:247], v[64:79]\n"
      "v_mov_b32 v96, -1\n"
      "v_mov_b32 v97, -1\n"
      "v_mov_b32 v98, -1\n"
      "v_mov_b32 v99, -1\n"
      "buffer_store_dwordx4 v[96:99], v14, s[64:67], s62 offen offset:0 sc1\n"
      "buffer_store_dwordx4 v[96:99], v14, s[64:67], s62 offen offset:1024 sc1\n"
      "s_waitcnt vmcnt(12)\n"
      "s_add_u32 m0, s39, 16384\n"
      "s_add_u32 s40, s40, 0x8000\n"
      "s_and_b32 s40, s40, 0xfffff\n"
      "buffer_load_dwordx4 v1, s[20:23], s40 offen  lds\n"
      "s_waitcnt vmcnt(12)\n"
      "s_add_u32 m0, s39, 18432\n"
      "s_add_u32 s40, s40, 0x8000\n"
      "s_and_b32 s40, s40, 0xfffff\n"
      "buffer_load_dwordx4 v1, s[20:23], s40 offen  lds\n"
      "s_barrier\n"
      "ds_read_b64_tr_b16 a[244:245], v8 offset:28672\n"
      "ds_read_b64_tr_b16 a[246:247], v8 offset:29184\n"
      "ds_read_b64_tr_b16 a[240:241], v3 offset:28672\n"
      "ds_read_b64_tr_b16 a[242:243], v3 offset:29184\n"
      "ds_read_b128 v[248:251], v11 offset:12288\n"
      "ds_read_b64_tr_b16 a[252:253], v8 offset:30720\n"
      "ds_read_b64_tr_b16 a[254:255], v8 offset:31232\n"
      "ds_read_b64_tr_b16 a[248:249], v3 offset:30720\n"
      "ds_read_b64_tr_b16 a[250:251], v3 offset:31232\n"
      "ds_read_b128 v[252:255], v12 offset:12288\n"
      "s_waitcnt lgkmcnt(5)\n"
      "v_mfma_f32_32x32x16_bf16 v[80:95], a[244:247], v[248:251], v[80:95]\n"
      "v_mfma_f32_32x32x16_bf16 v[64:79], a[240:243], v[248:251], v[64:79]\n"
      "s_waitcnt lgkmcnt(0)\n"
      "v_mfma_f32_32x32x16_bf16 v[80:95], a[252:255], v[252:255], v[80:95]\n"
      "v_mfma_f32_32x32x16_bf16 v[64:79], a[248:251], v[252:255], v[64:79]\n"
      "s_add_u32 m0, s39, 20480\n"
      "s_add_u32 s40, s40, 0x8000\n"
      "s_and_b32 s40, s40, 0xfffff\n"
      "buffer_load_dwordx4 v1, s[20:23], s40 offen  lds\n"
      "s_nop 7\n"
      "ds_write_b128 v16, v[80:83] offset:0\n"
      "ds_write_b128 v16, v[84:87] offset:1024\n"
      "ds_write_b128 v16, v[88:91] offset:2048\n"
      "ds_write_b128 v16, v[92:95] offset:3072\n"
      "s_add_u32 m0, s39, 22528\n"
      "s_add_u32 s40, s40, 0x8000\n"
      "s_and_b32 s40, s40, 0xfffff\n"
      "buffer_load_dwordx4 v1, s[20:23], s40 offen  lds\n"
      "s_cmp_lg_u32 s36, 0\n"
      "s_cbranch_scc1 L_h4\n"
      "s_waitcnt vmcnt(7)\n"
      "s_or_b32 s53, s47, 1\n"
      "v_cmp_ne_u32 vcc, s53, v29\n"
      "s_cbranch_vccnz L_hs_slow\n"
      "L_hs_done:\n"
      "buffer_load_dword v29, v24, s[64:67], 0 offen sc1\n"
      "L_h4:\n"
      "v_pk_fma_f32 v[64:65], v[32:33], v[64:65], v[48:49]\n"
      "v_pk_fma_f32 v[66:67], v[34:35], v[66:67], v[50:51]\n"
      "v_pk_fma_f32 v[68:69], v[36:37], v[68:69], v[52:53]\n"
      "v_pk_fma_f32 v[70:71], v[38:39], v[70:71], v[54:55]\n"
      "v_pk_fma_f32 v[72:73], v[40:41], v[72:73], v[56:57]\n"
      "v_pk_fma_f32 v[74:75], v[42:43], v[74:75], v[58:59]\n"
      "v_pk_fma_f32 v[76:77], v[44:45], v[76:77], v[60:61]\n"
      "v_pk_fma_f32 v[78:79], v[46:47], v[78:79], v[62:63]\n"
      "s_waitcnt lgkmcnt(0)\n"
      "s_barrier\n"
      "ds_read_b128 v[128:131], v17 offset:0\n"
      "ds_read_b128 v[132:135], v17 offset:1024\n"
      "ds_read_b128 v[136:139], v17 offset:2048\n"
      "ds_read_b128 v[140:143], v17 offset:3072\n"
      "s_add_u32 m0, s39, 24576\n"
      "s_add_u32 s40, s40, 0x8000\n"
      "s_and_b32 s40, s40, 0xfffff\n"
      "buffer_load_dwordx4 v1, s[20:23], s40 offen  lds\n"
      "s_add_u32 m0, s39, 26624\n"
      "s_add_u32 s40, s40, 0x8000\n"
      "s_and_b32 s40, s40, 0xfffff\n"
      "buffer_load_dwordx4 v1, s[20:23], s40 offen  lds\n"
      "s_waitcnt lgkmcnt(3)\n"
      "v_pk_fma_f32 v[64:65], v[32:33], v[128:129], v[64:65]\n"
      "v_pk_fma_f32 v[66:67], v[34:35], v[130:131], v[66:67]\n"
      "s_waitcnt lgkmcnt(2)\n"
      "v_pk_fma_f32 v[68:69], v[36:37], v[132:133], v[68:69]\n"
      "v_pk_fma_f32 v[70:71], v[38:39], v[134:135], v[70:71]\n"
      "v_cvt_pk_bf16_f32 v120, v64, v65\n"
      "v_cvt_pk_bf16_f32 v121, v66, v67\n"
      "v_cvt_pk_bf16_f32 v122, v68, v69\n"
      "v_cvt_pk_bf16_f32 v123, v70, v71\n"
      "v_min_u32 v120, -2, v120\n"
      "v_min_u32 v121, -2, v121\n"
      "v_min_u32 v122, -2, v122\n"
      "v_min_u32 v123, -2, v123\n"
      "s_waitcnt vmcnt(6)\n"
      "buffer_store_dwordx4 v[120:123], v14, s[64:67], 0 offen offset:0 sc1\n"
      "s_waitcnt lgkmcnt(1)\n"
      "v_pk_fma_f32 v[72:73], v[40:41], v[136:137], v[72:73]\n"
      "v_pk_fma_f32 v[74:75], v[42:43], v[138:139], v[74:75]\n"
      "s_waitcnt lgkmcnt(0)\n"
      "v_pk_fma_f32 v[76:77], v[44:45], v[140:141], v[76:77]\n"
      "v_pk_fma_f32 v[78:79], v[46:47], v[142:143], v[78:79]\n"
      "v_cvt_pk_bf16_f32 v124, v72, v73\n"
      "v_cvt_pk_bf16_f32 v125, v74, v75\n"
      "v_cvt_pk_bf16_f32 v126, v76, v77\n"
      "v_cvt_pk_bf16_f32 v127, v78, v79\n"
      "v_min_u32 v124, -2, v124\n"
      "v_min_u32 v125, -2, v125\n"
      "v_min_u32 v126, -2, v126\n"
      "v_min_u32 v127, -2, v127\n"
      "buffer_store_dwordx4 v[124:127], v14, s[64:67], 0 offen offset:1024 sc1\n"
      "s_waitcnt vmcnt(18)\n"
      "s_waitcnt vmcnt(17)\n"
      "s_waitcnt vmcnt(16)\n"
      "v_lshlrev_b32 v48, 16, v120\n"
      "v_and_b32 v49, 0xffff0000, v120\n"
      "v_lshlrev_b32 v224, 16, v104\n"
      "v_and_b32 v225, 0xffff0000, v104\n"
      "s_waitcnt vmcnt(15)\n"
      "s_add_u32 m0, s39, 28672\n"
      "s_add_u32 s40, s40, 0x8000\n"
      "s_and_b32 s40, s40, 0xfffff\n"
      "buffer_load_dwordx4 v1, s[20:23], s40 offen  lds\n"
      "v_pk_fma_f32 v[48:49], v[32:33], v[224:225], v[48:49]\n"
      "v_lshlrev_b32 v50, 16, v121\n"
      "v_and_b32 v51, 0xffff0000, v121\n"
      "v_lshlrev_b32 v226, 16, v105\n"
      "s_barrier\n"
      "ds_read_b64_tr_b16 a[4:5], v8 offset:0\n"
      "ds_read_b64_tr_b16 a[6:7], v8 offset:512\n"
      "ds_read_b64_tr_b16 a[0:1], v3 offset:0\n"
      "ds_read_b64_tr_b16 a[2:3], v3 offset:512\n"
      "v_and_b32 v227, 0xffff0000, v105\n"
      "v_pk_fma_f32 v[50:51], v[34:35], v[226:227], v[50:51]\n"
      "v_lshlrev_b32 v52, 16, v122\n"
      "v_and_b32 v53, 0xffff0000, v122\n"
      "s_waitcnt vmcnt(15)\n"
      "s_add_u32 m0, s39, 30720\n"
      "s_add_u32 s40, s40, 0x8000\n"
      "s_and_b32 s40, s40, 0xfffff\n"
      "buffer_load_dwordx4 v1, s[20:23], s40 offen  lds\n"
      "ds_read_b64_tr_b16 a[12:13], v8 offset:2048\n"
      "ds_read_b64_tr_b16 a[14:15], v8 offset:2560\n"
      "ds_read_b64_tr_b16 a[8:9], v3 offset:2048\n"
      "ds_read_b64_tr_b16 a[10:11], v3 offset:2560\n"
      "v_lshlrev_b32 v228, 16, v106\n"
      "v_and_b32 v229, 0xffff0000, v106\n"
      "v_pk_fma_f32 v[52:53], v[36:37], v[228:229], v[52:53]\n"
      "v_lshlrev_b32 v54, 16, v123\n"
      "s_waitcnt vmcnt(15)\n"
      "s_add_u32 m0, s39, 32768\n"
      "s_add_u32 s40, s40, 0x8000\n"
      "s_and_b32 s40, s40, 0xfffff\n"
      "buffer_load_dwordx4 v1, s[20:23], s40 offen  lds\n"
      "ds_read_b64_tr_b16 a[20:21], v8 offset:4096\n"
      "ds_read_b64_tr_b16 a[22:23], v8 offset:4608\n"
      "ds_read_b64_tr_b16 a[16:17], v3 offset:4096\n"
      "ds_read_b64_tr_b16 a[18:19], v3 offset:4608\n"
      "v_and_b32 v55, 0xffff0000, v123\n"
      "v_lshlrev_b32 v230, 16, v107\n"
      "v_and_b32 v231, 0xffff0000, v107\n"
      "v_pk_fma_f32 v[54:55], v[38:39], v[230:231], v[54:55]\n"
      "s_waitcnt vmcnt(15)\n"
      "s_add_u32 m0, s39, 34816\n"
      "s_add_u32 s40, s40, 0x8000\n"
      "s_and_b32 s40, s40, 0xfffff\n"
      "buffer_load_dwordx4 v1, s[20:23], s40 offen  lds\n"
      "s_cmp_lg_u32 s36, 0\n"
      "s_cbranch_scc1 L_h5\n"
      "s_waitcnt vmcnt(8)\n"
      "v_cmp_ne_u32 vcc, v28, v29\n"
      "s_cbranch_vccnz L_hs_resend\n"
      "L_hs_resent:\n"
      "L_h5:\n"
      "s_waitcnt lgkmcnt(8)\n"
      "ds_read_b64_tr_b16 a[28:29], v8 offset:6144\n"
      "ds_read_b64_tr_b16 a[30:31], v8 offset:6656\n"
      "ds_read_b64_tr_b16 a[24:25], v3 offset:6144\n"
      "ds_read_b64_tr_b16 a[26:27], v3 offset:6656\n"
      "v_lshlrev_b32 v56, 16, v124\n"
      "v_and_b32 v57, 0xffff0000, v124\n"
      "v_lshlrev_b32 v232, 16, v108\n"
      "v_and_b32 v233, 0xffff0000, v108\n"
      "s_waitcnt vmcnt(15)\n"
      "s_add_u32 m0, s39, 36864\n"
      "s_add_u32 s40, s40, 0x8000\n"
      "s_and_b32 s40, s40, 0xfffff\n"
      "buffer_load_dwordx4 v1, s[20:23], s40 offen  lds\n"
      "s_waitcnt lgkmcnt(0)\n"
      "s_barrier\n"
      "ds_read_b64_tr_b16 a[36:37], v8 offset:8192\n"
      "ds_read_b64_tr_b16 a[38:39], v8 offset:8704\n"
      "ds_read_b64_tr_b16 a[32:33], v3 offset:8192\n"
      "ds_read_b64_tr_b16 a[34:35], v3 offset:8704\n"
      "v_pk_fma_f32 v[56:57], v[40:41], v[232:233], v[56:57]\n"
      "v_lshlrev_b32 v58, 16, v125\n"
      "v_and_b32 v59, 0xffff0000, v125\n"
      "v_lshlrev_b32 v234, 16, v109\n"
      "s_waitcnt vmcnt(12)\n"
      "s_add_u32 m0, s39, 38912\n"
      "s_add_u32 s40, s40, 0x8000\n"
      "s_and_b32 s40, s40, 0xfffff\n"
      "buffer_load_dwordx4 v1, s[20:23], s40 offen  lds\n"
      "ds_read_b64_tr_b16 a[44:45], v8 offset:10240\n"
      "ds_read_b64_tr_b16 a[46:47], v8 offset:10752\n"
      "ds_read_b64_tr_b16 a[40:41], v3 offset:10240\n"
      "ds_read_b64_tr_b16 a[42:43], v3 offset:10752\n"
      "v_and_b32 v235, 0xffff0000, v109\n"
      "v_pk_fma_f32 v[58:59], v[42:43], v[234:235], v[58:59]\n"
      "v_lshlrev_b32 v60, 16, v126\n"
      "v_and_b32 v61, 0xffff0000, v126\n"
      "s_waitcnt vmcnt(12)\n"
      "s_add_u32 m0, s39, 40960\n"
      "s_add_u32 s40, s40, 0x8000\n"
      "s_and_b32 s40, s40, 0xfffff\n"
      "buffer_load_dwordx4 v1, s[20:23], s40 offen  lds\n"
      "ds_read_b64_tr_b16 a[52:53], v8 offset:12288\n"
      "ds_read_b64_tr_b16 a[54:55], v8 offset:12800\n"
      "ds_read_b64_tr_b16 a[48:49], v3 offset:12288\n"
      "ds_read_b64_tr_b16 a[50:51], v3 offset:12800\n"
      "v_lshlrev_b32 v236, 16, v110\n"
      "v_and_b32 v237, 0xffff0000, v110\n"
      "v_pk_fma_f32 v[60:61], v[44:45], v[236:237], v[60:61]\n"
      "v_lshlrev_b32 v62, 16, v127\n"
      "s_waitcnt vmcnt(12)\n"
      "s_add_u32 m0, s39, 43008\n"
      "s_add_u32 s40, s40, 0x8000\n"
      "s_and_b32 s40, s40, 0xfffff\n"
      "buffer_load_dwordx4 v1, s[20:23], s40 offen  lds\n"
      "s_waitcnt lgkmcnt(8)\n"
      "ds_read_b64_tr_b16 a[60:61], v8 offset:14336\n"
      "ds_read_b64_tr_b16 a[62:63], v8 offset:14848\n"
      "ds_read_b64_tr_b16 a[56:57], v3 offset:14336\n"
      "ds_read_b64_tr_b16 a[58:59], v3 offset:14848\n"
      "v_and_b32 v63, 0xffff0000, v127\n"
      "v_lshlrev_b32 v238, 16, v111\n"
      "v_and_b32 v239, 0xffff0000, v111\n"
      "v_pk_fma_f32 v[62:63], v[46:47], v[238:239], v[62:63]\n"
      "s_waitcnt vmcnt(12)\n"
      "s_add_u32 m0, s39, 45056\n"
      "s_add_u32 s40, s40, 0x8000\n"
      "s_and_b32 s40, s40, 0xfffff\n"
      "buffer_load_dwordx4 v1, s[20:23], s40 offen  lds\n"
      "s_waitcnt lgkmcnt(0)\n"
      "s_barrier\n"
      "ds_read_b64_tr_b16 a[68:69], v8 offset:16384\n"
      "ds_read_b64_tr_b16 a[70:71], v8 offset:16896\n"
      "ds_read_b64_tr_b16 a[64:65], v3 offset:16384\n"
      "ds_read_b64_tr_b16 a[66:67], v3 offset:16896\n"
      "s_waitcnt vmcnt(12)\n"
      "s_add_u32 m0, s39, 47104\n"
      "s_add_u32 s40, s40, 0x8000\n"
      "s_and_b32 s40, s40, 0xfffff\n"
      "buffer_load_dwordx4 v1, s[20:23], s40 offen  lds\n"
      "ds_read_b64_tr_b16 a[76:77], v8 offset:18432\n"
      "ds_read_b64_tr_b16 a[78:79], v8 offset:18944\n"
      "ds_read_b64_tr_b16 a[72:73], v3 offset:18432\n"
      "ds_read_b64_tr_b16 a[74:75], v3 offset:18944\n"
      "s_waitcnt vmcnt(12)\n"
      "s_add_u32 m0, s39, 49152\n"
      "s_add_u32 s40, s40, 0x8000\n"
      "s_and_b32 s40, s40, 0xfffff\n"
      "buffer_load_dwordx4 v1, s[20:23], s40 offen  lds\n"
      "ds_read_b64_tr_b16 a[84:85], v8 offset:20480\n"
      "ds_read_b64_tr_b16 a[86:87], v8 offset:20992\n"
      "ds_read_b64_tr_b16 a[80:81], v3 offset:20480\n"
      "ds_read_b64_tr_b16 a[82:83], v3 offset:20992\n"
      "s_waitcnt vmcnt(10)\n"
      "s_add_u32 m0, s39, 51200\n"
      "s_add_u32 s40, s40, 0x8000\n"
      "s_and_b32 s40, s40, 0xfffff\n"
      "buffer_load_dwordx4 v1, s[20:23], s40 offen  lds\n"
      "s_waitcnt lgkmcnt(8)\n"
      "ds_read_b64_tr_b16 a[92:93], v8 offset:22528\n"
      "ds_read_b64_tr_b16 a[94:95], v8 offset:23040\n"
      "ds_read_b64_tr_b16 a[88:89], v3 offset:22528\n"
      "ds_read_b64_tr_b16 a[90:91], v3 offset:23040\n"
      "s_waitcnt vmcnt(10)\n"
      "s_add_u32 m0, s39, 53248\n"
      "s_add_u32 s40, s40, 0x8000\n"
      "s_and_b32 s40, s40, 0xfffff\n"
      "buffer_load_dwordx4 v1, s[20:23], s40 offen  lds\n"
      "s_waitcnt lgkmcnt(0)\n"
      "s_barrier\n"
      "ds_read_b64_tr_b16 a[100:101], v8 offset:24576\n"
      "ds_read_b64_tr_b16 a[102:103], v8 offset:25088\n"
      "ds_read_b64_tr_b16 a[96:97], v3 offset:24576\n"
      "ds_read_b64_tr_b16 a[98:99], v3 offset:25088\n"
      "s_waitcnt vmcnt(10)\n"
      "s_add_u32 m0, s39, 55296\n"
      "s_add_u32 s40, s40, 0x8000\n"
      "s_and_b32 s40, s40, 0xfffff\n"
      "buffer_load_dwordx4 v1, s[20:23], s40 offen  lds\n"
      "ds_read_b64_tr_b16 a[108:109], v8 offset:26624\n"
      "ds_read_b64_tr_b16 a[110:111], v8 offset:27136\n"
      "ds_read_b64_tr_b16 a[104:105], v3 offset:26624\n"
      "ds_read_b64_tr_b16 a[106:107], v3 offset:27136\n"
      "s_waitcnt vmcnt(10)\n"
      "s_add_u32 m0, s39, 57344\n"
      "s_add_u32 s40, s40, 0x8000\n"
      "s_and_b32 s40, s40, 0xfffff\n"
      "buffer_load_dwordx4 v1, s[20:23], s40 offen  lds\n"
      "ds_read_b64_tr_b16 a[116:117], v8 offset:28672\n"
      "ds_read_b64_tr_b16 a[118:119], v8 offset:29184\n"
      "ds_read_b64_tr_b16 a[112:113], v3 offset:28672\n"
      "ds_read_b64_tr_b16 a[114:115], v3 offset:29184\n"
      "s_waitcnt vmcnt(10)\n"
      "s_add_u32 m0, s39, 59392\n"
      "s_add_u32 s40, s40, 0x8000\n"
      "s_and_b32 s40, s40, 0xfffff\n"
      "buffer_load_dwordx4 v1, s[20:23], s40 offen  lds\n"
      "s_waitcnt lgkmcnt(8)\n"
      "ds_read_b64_tr_b16 a[124:125], v8 offset:30720\n"
      "ds_read_b64_tr_b16 a[126:127], v8 offset:31232\n"
      "ds_read_b64_tr_b16 a[120:121], v3 offset:30720\n"
      "ds_read_b64_tr_b16 a[122:123], v3 offset:31232\n"
      "s_waitcnt vmcnt(10)\n"
      "s_add_u32 m0, s39, 61440\n"
      "s_add_u32 s40, s40, 0x8000\n"
      "s_and_b32 s40, s40, 0xfffff\n"
      "buffer_load_dwordx4 v1, s[20:23], s40 offen  lds\n"
      "s_waitcnt lgkmcnt(0)\n"
      "s_barrier\n"
      "ds_read_b64_tr_b16 a[132:133], v8 offset:32768\n"
      "ds_read_b64_tr_b16 a[134:135], v8 offset:33280\n"
      "ds_read_b64_tr_b16 a[128:129], v3 offset:32768\n"
      "ds_read_b64_tr_b16 a[130:131], v3 offset:33280\n"
      "s_waitcnt vmcnt(10)\n"
      "s_add_u32 m0, s39, 63488\n"
      "s_add_u32 s40, s40, 0x8000\n"
      "s_and_b32 s40, s40, 0xfffff\n"
      "buffer_load_dwordx4 v1, s[20:23], s40 offen  lds\n"
      "ds_read_b64_tr_b16 a[140:141], v8 offset:34816\n"
      "ds_read_b64_tr_b16 a[142:143], v8 offset:35328\n"
      "ds_read_b64_tr_b16 a[136:137], v3 offset:34816\n"
      "ds_read_b64_tr_b16 a[138:139], v3 offset:35328\n"
      "s_waitcnt vmcnt(10)\n"
      "ds_read_b64_tr_b16 a[148:149], v8 offset:36864\n"
      "ds_read_b64_tr_b16 a[150:151], v8 offset:37376\n"
      "ds_read_b64_tr_b16 a[144:145], v3 offset:36864\n"
      "ds_read_b64_tr_b16 a[146:147], v3 offset:37376\n"
      "s_waitcnt vmcnt(9)\n"
      "s_waitcnt lgkmcnt(8)\n"
      "ds_read_b64_tr_b16 a[156:157], v8 offset:38912\n"
      "ds_read_b64_tr_b16 a[158:159], v8 offset:39424\n"
      "ds_read_b64_tr_b16 a[152:153], v3 offset:38912\n"
      "ds_read_b64_tr_b16 a[154:155], v3 offset:39424\n"
      "s_waitcnt vmcnt(8)\n"
      "s_waitcnt lgkmcnt(0)\n"
      "s_barrier\n"
      "ds_read_b64_tr_b16 a[164:165], v8 offset:40960\n"
      "ds_read_b64_tr_b16 a[166:167], v8 offset:41472\n"
      "ds_read_b64_tr_b16 a[160:161], v3 offset:40960\n"
      "ds_read_b64_tr_b16 a[162:163], v3 offset:41472\n"
      "s_waitcnt vmcnt(7)\n"
      "ds_read_b64_tr_b16 a[172:173], v8 offset:43008\n"
      "ds_read_b64_tr_b16 a[174:175], v8 offset:43520\n"
      "ds_read_b64_tr_b16 a[168:169], v3 offset:43008\n"
      "ds_read_b64_tr_b16 a[170:171], v3 offset:43520\n"
      "s_waitcnt vmcnt(6)\n"
      "s_lshl_b32 s41, s49, 10\n"
      "buffer_load_dwordx4 v[128:131], v13, s[64:67], s41 offen sc1\n"
      "s_add_u32 s41, s41, 0x400\n"
      "s_and_b32 s41, s41, 0x7fff\n"
      "buffer_load_dwordx4 v[132:135], v13, s[64:67], s41 offen sc1\n"
      "s_add_u32 s41, s41, 0x400\n"
      "s_and_b32 s41, s41, 0x7fff\n"
      "buffer_load_dwordx4 v[136:139], v13, s[64:67], s41 offen sc1\n"
      "s_add_u32 s41, s41, 0x400\n"
      "s_and_b32 s41, s41, 0x7fff\n"
      "buffer_load_dwordx4 v[140:143], v13, s[64:67], s41 offen sc1\n"
      "ds_read_b64_tr_b16 a[180:181], v8 offset:45056\n"
      "ds_read_b64_tr_b16 a[182:183], v8 offset:45568\n"
      "ds_read_b64_tr_b16 a[176:177], v3 offset:45056\n"
      "ds_read_b64_tr_b16 a[178:179], v3 offset:45568\n"
      "s_waitcnt vmcnt(9)\n"
      "s_add_u32 s41, s41, 0x400\n"
      "s_and_b32 s41, s41, 0x7fff\n"
      "buffer_load_dwordx4 v[144:147], v13, s[64:67], s41 offen sc1\n"
      "s_waitcnt lgkmcnt(8)\n"
      "ds_read_b64_tr_b16 a[188:189], v8 offset:47104\n"
      "ds_read_b64_tr_b16 a[190:191], v8 offset:47616\n"
      "ds_read_b64_tr_b16 a[184:185], v3 offset:47104\n"
      "ds_read_b64_tr_b16 a[186:187], v3 offset:47616\n"
      "s_waitcnt vmcnt(9)\n"
      "s_add_u32 s41, s41, 0x400\n"
      "s_and_b32 s41, s41, 0x7fff\n"
      "buffer_load_dwordx4 v[148:151], v13, s[64:67], s41 offen sc1\n"
      "s_waitcnt lgkmcnt(0)\n"
      "s_barrier\n"
      "ds_read_b64_tr_b16 a[196:197], v8 offset:49152\n"
      "ds_read_b64_tr_b16 a[198:199], v8 offset:49664\n"
      "ds_read_b64_tr_b16 a[192:193], v3 offset:49152\n"
      "ds_read_b64_tr_b16 a[194:195], v3 offset:49664\n"
      "s_waitcnt vmcnt(9)\n"
      "s_add_u32 s41, s41, 0x400\n"
      "s_and_b32 s41, s41, 0x7fff\n"
      "buffer_load_dwordx4 v[152:155], v13, s[64:67], s41 offen sc1\n"
      "ds_read_b64_tr_b16 a[204:205], v8 offset:51200\n"
      "ds_read_b64_tr_b16 a[206:207], v8 offset:51712\n"
      "ds_read_b64_tr_b16 a[200:201], v3 offset:51200\n"
      "ds_read_b64_tr_b16 a[202:203], v3 offset:51712\n"
      "s_waitcnt vmcnt(9)\n"
      "s_add_u32 s41, s41, 0x400\n"
      "s_and_b32 s41, s41, 0x7fff\n"
      "buffer_load_dwordx4 v[156:159], v13, s[64:67], s41 offen sc1\n"
      "ds_read_b64_tr_b16 a[212:213], v8 offset:53248\n"
      "ds_read_b64_tr_b16 a[214:215], v8 offset:53760\n"
      "ds_read_b64_tr_b16 a[208:209], v3 offset:53248\n"
      "ds_read_b64_tr_b16 a[210:211], v3 offset:53760\n"
      "s_waitcnt vmcnt(9)\n"
      "s_add_u32 s41, s41, 0x400\n"
      "s_and_b32 s41, s41, 0x7fff\n"
      "buffer_load_dwordx4 v[160:163], v13, s[64:67], s41 offen sc1\n"
      "s_waitcnt lgkmcnt(8)\n"
      "ds_read_b64_tr_b16 a[220:221], v8 offset:55296\n"
      "ds_read_b64_tr_b16 a[222:223], v8 offset:55808\n"
      "ds_read_b64_tr_b16 a[216:217], v3 offset:55296\n"
      "ds_read_b64_tr_b16 a[218:219], v3 offset:55808\n"
      "s_waitcnt vmcnt(9)\n"
      "s_add_u32 s41, s41, 0x400\n"
      "s_and_b32 s41, s41, 0x7fff\n"
      "buffer_load_dwordx4 v[164:167], v13, s[64:67], s41 offen sc1\n"
      "s_waitcnt lgkmcnt(0)\n"
      "s_barrier\n"
      ".p2align 5\n"
      "s_waitcnt vmcnt(9)\n"
      "s_add_u32 s41, s41, 0x400\n"
      "s_and_b32 s41, s41, 0x7fff\n"
      "buffer_load_dwordx4 v[168:171], v13, s[64:67], s41 offen sc1\n"
      "v_max3_u32 v22, v128, v129, v130\n"
      "v_max_u32 v22, v22, v131\n"
      "v_cmp_eq_u32 vcc, -1, v22\n"
      "s_cbranch_vccnz L_f1_0\n"
      "L_k1_0:\n"
      "s_waitcnt vmcnt(9)\n"
      "v_mfma_f32_32x32x16_bf16 v[80:95], a[4:7], v[128:131], 0\n"
      "v_max3_u32 v22, v132, v133, v134\n"
      "s_add_u32 s41, s41, 0x400\n"
      "s_and_b32 s41, s41, 0x7fff\n"
      "v_max_u32 v22, v22, v135\n"
      "v_mfma_f32_32x32x16_bf16 v[64:79], a[0:3], v[128:131], 0\n"
      "v_cmp_eq_u32 vcc, -1, v22\n"
      "buffer_load_dwordx4 v[172:175], v13, s[64:67], s41 offen sc1\n"
      "s_cbranch_vccnz L_f1_1\n"
      "L_k1_1:\n"
      "s_waitcnt vmcnt(9)\n"
      "v_mfma_f32_32x32x16_bf16 v[80:95], a[12:15], v[132:135], v[80:95]\n"
      "v_max3_u32 v22, v136, v137, v138\n"
      "s_add_u32 s41, s41, 0x400\n"
      "s_and_b32 s41, s41, 0x7fff\n"
      "v_max_u32 v22, v22, v139\n"
      "v_mfma_f32_32x32x16_bf16 v[64:79], a[8:11], v[132:135], v[64:79]\n"
      "v_cmp_eq_u32 vcc, -1, v22\n"
      "buffer_load_dwordx4 v[176:179], v13, s[64:67], s41 offen sc1\n"
      "s_cbranch_vccnz L_f1_2\n"
      "L_k1_2:\n"
      "s_waitcnt vmcnt(9)\n"
      "v_mfma_f32_32x32x16_bf16 v[80:95], a[20:23], v[136:139], v[80:95]\n"
      "v_max3_u32 v22, v140, v141, v142\n"
      "s_add_u32 s41, s41, 0x400\n"
      "s_and_b32 s41, s41, 0x7fff\n"
      "v_max_u32 v22, v22, v143\n"
      "v_mfma_f32_32x32x16_bf16 v[64:79], a[16:19], v[136:139], v[64:79]\n"
      "v_cmp_eq_u32 vcc, -1, v22\n"
      "buffer_load_dwordx4 v[180:183], v13, s[64:67], s41 offen sc1\n"
      "s_cbranch_vccnz L_f1_3\n"
      "L_k1_3:\n"
      "s_waitcnt vmcnt(9)\n"
      "v_mfma_f32_32x32x16_bf16 v[80:95], a[28:31], v[140:143], v[80:95]\n"
      "v_max3_u32 v22, v144, v145, v146\n"
      "s_add_u32 s41, s41, 0x400\n"
      "s_and_b32 s41, s41, 0x7fff\n"
      "v_max_u32 v22, v22, v147\n"
      "v_mfma_f32_32x32x16_bf16 v[64:79], a[24:27], v[140:143], v[64:79]\n"
      "v_cmp_eq_u32 vcc, -1, v22\n"
      "buffer_load_dwordx4 v[184:187], v13, s[64:67], s41 offen sc1\n"
      "s_cbranch_vccnz L_f1_4\n"
      "L_k1_4:\n"
      "s_waitcnt vmcnt(9)\n"
      "v_mfma_f32_32x32x16_bf16 v[80:95], a[36:39], v[144:147], v[80:95]\n"
      "v_max3_u32 v22, v148, v149, v150\n"
      "s_add_u32 s41, s41, 0x400\n"
      "s_and_b32 s41, s41, 0x7fff\n"
      "v_max_u32 v22, v22, v151\n"
      "v_mfma_f32_32x32x16_bf16 v[64:79], a[32:35], v[144:147], v[64:79]\n"
      "v_cmp_eq_u32 vcc, -1, v22\n"
      "buffer_load_dwordx4 v[188:191], v13, s[64:67], s41 offen sc1\n"
      "s_cbranch_vccnz L_f1_5\n"
      "L_k1_5:\n"
      "s_waitcnt vmcnt(9)\n"
      "v_mfma_f32_32x32x16_bf16 v[80:95], a[44:47], v[148:151], v[80:95]\n"
      "v_max3_u32 v22, v152, v153, v154\n"
      "s_add_u32 s41, s41, 0x400\n"
      "s_and_b32 s41, s41, 0x7fff\n"
      "v_max_u32 v22, v22, v155\n"
      "v_mfma_f32_32x32x16_bf16 v[64:79], a[40:43], v[148:151], v[64:79]\n"
      "v_cmp_eq_u32 vcc, -1, v22\n"
      "buffer_load_dwordx4 v[192:195], v13, s[64:67], s41 offen sc1\n"
      "s_cbranch_vccnz L_f1_6\n"
      "L_k1_6:\n"
      "s_waitcnt vmcnt(9)\n"
      "v_mfma_f32_32x32x16_bf16 v[80:95], a[52:55], v[152:155], v[80:95]\n"
      "v_max3_u32 v22, v156, v157, v158\n"
      "s_add_u32 s41, s41, 0x400\n"
      "s_and_b32 s41, s41, 0x7fff\n"
      "v_max_u32 v22, v22, v159\n"
      "v_mfma_f32_32x32x16_bf16 v[64:79], a[48:51], v[152:155], v[64:79]\n"
      "v_cmp_eq_u32 vcc, -1, v22\n"
      "buffer_load_dwordx4 v[196:199], v13, s[64:67], s41 offen sc1\n"
      "s_cbranch_vccnz L_f1_7\n"
      "L_k1_7:\n"
      "s_waitcnt vmcnt(9)\n"
      "v_mfma_f32_32x32x16_bf16 v[80:95], a[60:63], v[156:159], v[80:95]\n"
      "v_max3_u32 v22, v160, v161, v162\n"
      "s_add_u32 s41, s41, 0x400\n"
      "s_and_b32 s41, s41, 0x7fff\n"
      "v_max_u32 v22, v22, v163\n"
      "v_mfma_f32_32x32x16_bf16 v[64:79], a[56:59], v[156:159], v[64:79]\n"
      "v_cmp_eq_u32 vcc, -1, v22\n"
      "buffer_load_dwordx4 v[200:203], v13, s[64:67], s41 offen sc1\n"
      "s_cbranch_vccnz L_f1_8\n"
      "L_k1_8:\n"
      "s_waitcnt vmcnt(9)\n"
      "v_mfma_f32_32x32x16_bf16 v[80:95], a[68:71], v[160:163], v[80:95]\n"
      "v_max3_u32 v22, v164, v165, v166\n"
      "s_add_u32 s41, s41, 0x400\n"
      "s_and_b32 s41, s41, 0x7fff\n"
      "v_max_u32 v22, v22, v167\n"
      "v_mfma_f32_32x32x16_bf16 v[64:79], a[64:67], v[160:163], v[64:79]\n"
      "v_cmp_eq_u32 vcc, -1, v22\n"
      "buffer_load_dwordx4 v[204:207], v13, s[64:67], s41 offen sc1\n"
      "s_cbranch_vccnz L_f1_9\n"
      "L_k1_9:\n"
      "s_waitcnt vmcnt(9)\n"
      "v_mfma_f32_32x32x16_bf16 v[80:95], a[76:79], v[164:167], v[80:95]\n"
      "v_max3_u32 v22, v168, v169, v170\n"
      "s_add_u32 s41, s41, 0x400\n"
      "s_and_b32 s41, s41, 0x7fff\n"
      "v_max_u32 v22, v22, v171\n"
      "v_mfma_f32_32x32x16_bf16 v[64:79], a[72:75], v[164:167], v[64:79]\n"
      "v_cmp_eq_u32 vcc, -1, v22\n"
      "buffer_load_dwordx4 v[208:211], v13, s[64:67], s41 offen sc1\n"
      "s_cbranch_vccnz L_f1_10\n"
      "L_k1_10:\n"
      "s_waitcnt vmcnt(9)\n"
      "v_mfma_f32_32x32x16_bf16 v[80:95], a[84:87], v[168:171], v[80:95]\n"
      "v_max3_u32 v22, v172, v173, v174\n"
      "s_add_u32 s41, s41, 0x400\n"
      "s_and_b32 s41, s41, 0x7fff\n"
      "v_max_u32 v22, v22, v175\n"
      "v_mfma_f32_32x32x16_bf16 v[64:79], a[80:83], v[168:171], v[64:79]\n"
      "v_cmp_eq_u32 vcc, -1, v22\n"
      "buffer_load_dwordx4 v[212:215], v13, s[64:67], s41 offen sc1\n"
      "s_cbranch_vccnz L_f1_11\n"
      "L_k1_11:\n"
      "v_mfma_f32_32x32x16_bf16 v[80:95], a[92:95], v[172:175], v[80:95]\n"
      "v_mfma_f32_32x32x16_bf16 v[64:79], a[88:91], v[172:175], v[64:79]\n"
      "s_waitcnt vmcnt(9)\n"
      "v_mfma_f32_32x32x16_bf16 v[80:95], a[100:103], v[176:179], v[80:95]\n"
      "s_add_u32 s41, s41, 0x400\n"
      "s_and_b32 s41, s41, 0x7fff\n"
      "v_mfma_f32_32x32x16_bf16 v[64:79], a[96:99], v[176:179], v[64:79]\n"
      "buffer_load_dwordx4 v[216:219], v13, s[64:67], s41 offen sc1\n"
      "s_waitcnt vmcnt(9)\n"
      "v_mfma_f32_32x32x16_bf16 v[80:95], a[108:111], v[180:183], v[80:95]\n"
      "s_add_u32 s41, s41, 0x400\n"
      "s_and_b32 s41, s41, 0x7fff\n"
      "ds_read_b64_tr_b16 a[228:229], v8 offset:57344\n"
      "ds_read_b64_tr_b16 a[230:231], v8 offset:57856\n"
      "v_mfma_f32_32x32x16_bf16 v[64:79], a[104:107], v[180:183], v[64:79]\n"
      "ds_read_b64_tr_b16 a[224:225], v3 offset:57344\n"
      "ds_read_b64_tr_b16 a[226:227], v3 offset:57856\n"
      "buffer_load_dwordx4 v[220:223], v13, s[64:67], s41 offen sc1\n"
      "s_waitcnt vmcnt(9)\n"
      "v_mfma_f32_32x32x16_bf16 v[80:95], a[116:119], v[184:187], v[80:95]\n"
      "s_add_u32 s41, s41, 0x400\n"
      "s_and_b32 s41, s41, 0x7fff\n"
      "ds_read_b64_tr_b16 a[236:237], v8 offset:59392\n"
      "ds_read_b64_tr_b16 a[238:239], v8 offset:59904\n"
      "v_mfma_f32_32x32x16_bf16 v[64:79], a[112:115], v[184:187], v[64:79]\n"
      "ds_read_b64_tr_b16 a[232:233], v3 offset:59392\n"
      "ds_read_b64_tr_b16 a[234:235], v3 offset:59904\n"
      "buffer_load_dwordx4 v[224:227], v13, s[64:67], s41 offen sc1\n"
      "s_waitcnt vmcnt(9)\n"
      "v_mfma_f32_32x32x16_bf16 v[80:95], a[124:127], v[188:191], v[80:95]\n"
      "s_add_u32 s41, s41, 0x400\n"
      "s_and_b32 s41, s41, 0x7fff\n"
      "ds_read_b64_tr_b16 a[244:245], v8 offset:61440\n"
      "ds_read_b64_tr_b16 a[246:247], v8 offset:61952\n"
      "v_mfma_f32_32x32x16_bf16 v[64:79], a[120:123], v[188:191], v[64:79]\n"
      "ds_read_b64_tr_b16 a[240:241], v3 offset:61440\n"
      "ds_read_b64_tr_b16 a[242:243], v3 offset:61952\n"
      "buffer_load_dwordx4 v[228:231], v13, s[64:67], s41 offen sc1\n"
      "s_waitcnt vmcnt(9)\n"
      "v_mfma_f32_32x32x16_bf16 v[80:95], a[132:135], v[192:195], v[80:95]\n"
      "s_add_u32 s41, s41, 0x400\n"
      "s_and_b32 s41, s41, 0x7fff\n"
      "s_waitcnt lgkmcnt(8)\n"
      "ds_read_b64_tr_b16 a[252:253], v8 offset:63488\n"
      "ds_read_b64_tr_b16 a[254:255], v8 offset:64000\n"
      "v_mfma_f32_32x32x16_bf16 v[64:79], a[128:131], v[192:195], v[64:79]\n"
      "ds_read_b64_tr_b16 a[248:249], v3 offset:63488\n"
      "ds_read_b64_tr_b16 a[250:251], v3 offset:64000\n"
      "buffer_load_dwordx4 v[232:235], v13, s[64:67], s41 offen sc1\n"
      "s_waitcnt vmcnt(9)\n"
      "v_mfma_f32_32x32x16_bf16 v[80:95], a[140:143], v[196:199], v[80:95]\n"
      "s_add_u32 s41, s41, 0x400\n"
      "s_and_b32 s41, s41, 0x7fff\n"
      "v_mfma_f32_32x32x16_bf16 v[64:79], a[136:139], v[196:199], v[64:79]\n"
      "buffer_load_dwordx4 v[236:239], v13, s[64:67], s41 offen sc1\n"
      "s_waitcnt vmcnt(9)\n"
      "v_mfma_f32_32x32x16_bf16 v[80:95], a[148:151], v[200:203], v[80:95]\n"
      "s_add_u32 s41, s41, 0x400\n"
      "s_and_b32 s41, s41, 0x7fff\n"
      "v_mfma_f32_32x32x16_bf16 v[64:79], a[144:147], v[200:203], v[64:79]\n"
      "buffer_load_dwordx4 v[240:243], v13, s[64:67], s41 offen sc1\n"
      "s_waitcnt vmcnt(9)\n"
      "v_mfma_f32_32x32x16_bf16 v[80:95], a[156:159], v[204:207], v[80:95]\n"
      "s_add_u32 s41, s41, 0x400\n"
      "s_and_b32 s41, s41, 0x7fff\n"
      "v_mfma_f32_32x32x16_bf16 v[64:79], a[152:155], v[204:207], v[64:79]\n"
      "buffer_load_dwordx4 v[244:247], v13, s[64:67], s41 offen sc1\n"
      "s_waitcnt vmcnt(9)\n"
      "v_mfma_f32_32x32x16_bf16 v[80:95], a[164:167], v[208:211], v[80:95]\n"
      "s_add_u32 s41, s41, 0x400\n"
      "s_and_b32 s41, s41, 0x7fff\n"
      "v_mfma_f32_32x32x16_bf16 v[64:79], a[160:163], v[208:211], v[64:79]\n"
      "buffer_load_dwordx4 v[248:251], v13, s[64:67], s41 offen sc1\n"
      "s_waitcnt vmcnt(9)\n"
      "v_mfma_f32_32x32x16_bf16 v[80:95], a[172:175], v[212:215], v[80:95]\n"
      "s_add_u32 s41, s41, 0x400\n"
      "s_and_b32 s41, s41, 0x7fff\n"
      "v_mfma_f32_32x32x16_bf16 v[64:79], a[168:171], v[212:215], v[64:79]\n"
      "buffer_load_dwordx4 v[252:255], v13, s[64:67], s41 offen sc1\n"
      "s_waitcnt vmcnt(9)\n"
      "v_mfma_f32_32x32x16_bf16 v[80:95], a[180:183], v[216:219], v[80:95]\n"
      "s_add_u32 m0, s39, 0\n"
      "s_lshl_b32 s40, s50, 15\n"
      "v_add_u32 v1, 0x200000, v1\n"
      "v_mfma_f32_32x32x16_bf16 v[64:79], a[176:179], v[216:219], v[64:79]\n"
      "buffer_load_dwordx4 v1, s[20:23], s40 offen  lds\n"
      "s_add_u32 s51, s63, 0x400000\n"
      "buffer_load_dword v0, v30, s[20:23], s51 offen\n"
      "s_waitcnt vmcnt(10)\n"
      "v_mfma_f32_32x32x16_bf16 v[80:95], a[188:191], v[220:223], v[80:95]\n"
      "s_add_u32 m0, s39, 2048\n"
      "s_add_u32 s40, s40, 0x8000\n"
      "s_and_b32 s40, s40, 0xfffff\n"
      "v_mfma_f32_32x32x16_bf16 v[64:79], a[184:187], v[220:223], v[64:79]\n"
      "buffer_load_dwordx4 v1, s[20:23], s40 offen  lds\n"
      "s_waitcnt vmcnt(10)\n"
      "v_mfma_f32_32x32x16_bf16 v[80:95], a[196:199], v[224:227], v[80:95]\n"
      "s_add_u32 m0, s39, 4096\n"
      "s_add_u32 s40, s40, 0x8000\n"
      "s_and_b32 s40, s40, 0xfffff\n"
      "v_mfma_f32_32x32x16_bf16 v[64:79], a[192:195], v[224:227], v[64:79]\n"
      "buffer_load_dwordx4 v1, s[20:23], s40 offen  lds\n"
      "s_waitcnt vmcnt(10)\n"
      "v_mfma_f32_32x32x16_bf16 v[80:95], a[204:207], v[228:231], v[80:95]\n"
      "s_add_u32 m0, s39, 6144\n"
      "s_add_u32 s40, s40, 0x8000\n"
      "s_and_b32 s40, s40, 0xfffff\n"
      "v_mfma_f32_32x32x16_bf16 v[64:79], a[200:203], v[228:231], v[64:79]\n"
      "buffer_load_dwordx4 v1, s[20:23], s40 offen  lds\n"
      "s_waitcnt vmcnt(10)\n"
      "v_mfma_f32_32x32x16_bf16 v[80:95], a[212:215], v[232:235], v[80:95]\n"
      "s_add_u32 m0, s39, 8192\n"
      "s_add_u32 s40, s40, 0x8000\n"
      "s_and_b32 s40, s40, 0xfffff\n"
      "v_mfma_f32_32x32x16_bf16 v[64:79], a[208:211], v[232:235], v[64:79]\n"
      "buffer_load_dwordx4 v1, s[20:23], s40 offen  lds\n"
      "s_waitcnt vmcnt(10)\n"
      "v_mfma_f32_32x32x16_bf16 v[80:95], a[220:223], v[236:239], v[80:95]\n"
      "s_add_u32 m0, s39, 10240\n"
      "s_add_u32 s40, s40, 0x8000\n"
      "s_and_b32 s40, s40, 0xfffff\n"
      "v_mfma_f32_32x32x16_bf16 v[64:79], a[216:219], v[236:239], v[64:79]\n"
      "buffer_load_dwordx4 v1, s[20:23], s40 offen  lds\n"
      "s_waitcnt vmcnt(10)\n"
      "s_waitcnt lgkmcnt(0)\n"
      "v_mfma_f32_32x32x16_bf16 v[80:95], a[228:231], v[240:243], v[80:95]\n"
      "s_add_u32 m0, s39, 12288\n"
      "s_add_u32 s40, s40, 0x8000\n"
      "s_and_b32 s40, s40, 0xfffff\n"
      "v_mfma_f32_32x32x16_bf16 v[64:79], a[224:227], v[240:243], v[64:79]\n"
      "buffer_load_dwordx4 v1, s[20:23], s40 offen  lds\n"
      "s_waitcnt vmcnt(10)\n"
      "v_mfma_f32_32x32x16_bf16 v[80:95], a[236:239], v[244:247], v[80:95]\n"
      "s_add_u32 m0, s39, 14336\n"
      "s_add_u32 s40, s40, 0x8000\n"
      "s_and_b32 s40, s40, 0xfffff\n"
      "v_mfma_f32_32x32x16_bf16 v[64:79], a[232:235], v[244:247], v[64:79]\n"
      "buffer_load_dwordx4 v1, s[20:23], s40 offen  lds\n"
      "s_waitcnt vmcnt(10)\n"
      "v_mfma_f32_32x32x16_bf16 v[80:95], a[244:247], v[248:251], v[80:95]\n"
      "s_add_u32 m0, s39, 16384\n"
      "s_add_u32 s40, s40, 0x8000\n"
      "s_and_b32 s40, s40, 0xfffff\n"
      "v_mfma_f32_32x32x16_bf16 v[64:79], a[240:243], v[248:251], v[64:79]\n"
      "buffer_load_dwordx4 v1, s[20:23], s40 offen  lds\n"
      "s_waitcnt vmcnt(10)\n"
      "v_mfma_f32_32x32x16_bf16 v[80:95], a[252:255], v[252:255], v[80:95]\n"
      "s_add_u32 m0, s39, 18432\n"
      "s_add_u32 s40, s40, 0x8000\n"
      "s_and_b32 s40, s40, 0xfffff\n"
      "v_mfma_f32_32x32x16_bf16 v[64:79], a[248:251], v[252:255], v[64:79]\n"
      "buffer_load_dwordx4 v1, s[20:23], s40 offen  lds\n"
      "s_add_u32 m0, s39, 20480\n"
      "s_add_u32 s40, s40, 0x8000\n"
      "s_and_b32 s40, s40, 0xfffff\n"
      "buffer_load_dwordx4 v1, s[20:23], s40 offen  lds\n"
      "s_nop 7\n"
      "v_cmp_u_f32 vcc, v80, v80\n"
      "s_cbranch_vccnz L_nan1\n"
      "L_nan1_x:\n"
      "ds_write_b128 v16, v[80:83] offset:0\n"
      "ds_write_b128 v16, v[84:87] offset:1024\n"
      "ds_write_b128 v16, v[88:91] offset:2048\n"
      "ds_write_b128 v16, v[92:95] offset:3072\n"
      "s_add_u32 m0, s39, 22528\n"
      "s_add_u32 s40, s40, 0x8000\n"
      "s_and_b32 s40, s40, 0xfffff\n"
      "buffer_load_dwordx4 v1, s[20:23], s40 offen  lds\n"
      "v_pk_fma_f32 v[64:65], v[32:33], v[64:65], v[48:49]\n"
      "v_pk_fma_f32 v[66:67], v[34:35], v[66:67], v[50:51]\n"
      "v_pk_fma_f32 v[68:69], v[36:37], v[68:69], v[52:53]\n"
      "v_pk_fma_f32 v[70:71], v[38:39], v[70:71], v[54:55]\n"
      "v_pk_fma_f32 v[72:73], v[40:41], v[72:73], v[56:57]\n"
      "v_pk_fma_f32 v[74:75], v[42:43], v[74:75], v[58:59]\n"
      "v_pk_fma_f32 v[76:77], v[44:45], v[76:77], v[60:61]\n"
      "v_pk_fma_f32 v[78:79], v[46:47], v[78:79], v[62:63]\n"
      "s_waitcnt lgkmcnt(0)\n"
      "s_barrier\n"
      "ds_read_b128 v[128:131], v17 offset:0\n"
      "ds_read_b128 v[132:135], v17 offset:1024\n"
      "ds_read_b128 v[136:139], v17 offset:2048\n"
      "ds_read_b128 v[140:143], v17 offset:3072\n"
      "s_add_u32 m0, s39, 24576\n"
      "s_add_u32 s40, s40, 0x8000\n"
      "s_and_b32 s40, s40, 0xfffff\n"
      "buffer_load_dwordx4 v1, s[20:23], s40 offen  lds\n"
      "s_add_u32 m0, s39, 26624\n"
      "s_add_u32 s40, s40, 0x8000\n"
      "s_and_b32 s40, s40, 0xfffff\n"
      "buffer_load_dwordx4 v1, s[20:23], s40 offen  lds\n"
      "s_waitcnt lgkmcnt(3)\n"
      "v_pk_fma_f32 v[64:65], v[32:33], v[128:129], v[64:65]\n"
      "v_pk_fma_f32 v[66:67], v[34:35], v[130:131], v[66:67]\n"
      "s_waitcnt lgkmcnt(2)\n"
      "v_pk_fma_f32 v[68:69], v[36:37], v[132:133], v[68:69]\n"
      "v_pk_fma_f32 v[70:71], v[38:39], v[134:135], v[70:71]\n"
      "v_cvt_pk_bf16_f32 v120, v64, v65\n"
      "v_cvt_pk_bf16_f32 v121, v66, v67\n"
      "v_cvt_pk_bf16_f32 v122, v68, v69\n"
      "v_cvt_pk_bf16_f32 v123, v70, v71\n"
      "v_min_u32 v120, -2, v120\n"
      "v_min_u32 v121, -2, v121\n"
      "v_min_u32 v122, -2, v122\n"
      "v_min_u32 v123, -2, v123\n"
      "buffer_store_dwordx4 v[120:123], v14, s[64:67], s62 offen offset:0 sc1\n"
      "s_waitcnt lgkmcnt(1)\n"
      "v_pk_fma_f32 v[72:73], v[40:41], v[136:137], v[72:73]\n"
      "v_pk_fma_f32 v[74:75], v[42:43], v[138:139], v[74:75]\n"
      "s_waitcnt lgkmcnt(0)\n"
      "v_pk_fma_f32 v[76:77], v[44:45], v[140:141], v[76:77]\n"
      "v_pk_fma_f32 v[78:79], v[46:47], v[142:143], v[78:79]\n"
      "v_cvt_pk_bf16_f32 v124, v72, v73\n"
      "v_cvt_pk_bf16_f32 v125, v74, v75\n"
      "v_cvt_pk_bf16_f32 v126, v76, v77\n"
      "v_cvt_pk_bf16_f32 v127, v78, v79\n"
      "v_min_u32 v124, -2, v124\n"
      "v_min_u32 v125, -2, v125\n"
      "v_min_u32 v126, -2, v126\n"
      "v_min_u32 v127, -2, v127\n"
      "buffer_store_dwordx4 v[124:127], v14, s[64:67], s62 offen offset:1024 sc1\n"
      "s_waitcnt vmcnt(16)\n"
      "s_waitcnt vmcnt(14)\n"
      "s_waitcnt vmcnt(13)\n"
      "v_lshlrev_b32 v48, 16, v120\n"
      "v_and_b32 v49, 0xffff0000, v120\n"
      "v_lshlrev_b32 v224, 16, v112\n"
      "v_and_b32 v225, 0xffff0000, v112\n"
      "s_waitcnt vmcnt(12)\n"
      "s_add_u32 m0, s39, 28672\n"
      "s_add_u32 s40, s40, 0x8000\n"
      "s_and_b32 s40, s40, 0xfffff\n"
      "buffer_load_dwordx4 v1, s[20:23], s40 offen  lds\n"
      "v_pk_fma_f32 v[48:49], v[32:33], v[224:225], v[48:49]\n"
      "v_lshlrev_b32 v50, 16, v121\n"
      "v_and_b32 v51, 0xffff0000, v121\n"
      "v_lshlrev_b32 v226, 16, v113\n"
      "s_barrier\n"
      "ds_read_b64_tr_b16 a[4:5], v8 offset:0\n"
      "ds_read_b64_tr_b16 a[6:7], v8 offset:512\n"
      "ds_read_b64_tr_b16 a[0:1], v3 offset:0\n"
      "ds_read_b64_tr_b16 a[2:3], v3 offset:512\n"
      "v_and_b32 v227, 0xffff0000, v113\n"
      "v_pk_fma_f32 v[50:51], v[34:35], v[226:227], v[50:51]\n"
      "v_lshlrev_b32 v52, 16, v122\n"
      "v_and_b32 v53, 0xffff0000, v122\n"
      "s_waitcnt vmcnt(12)\n"
      "s_add_u32 m0, s39, 30720\n"
      "s_add_u32 s40, s40, 0x8000\n"
      "s_and_b32 s40, s40, 0xfffff\n"
      "buffer_load_dwordx4 v1, s[20:23], s40 offen  lds\n"
      "ds_read_b64_tr_b16 a[12:13], v8 offset:2048\n"
      "ds_read_b64_tr_b16 a[14:15], v8 offset:2560\n"
      "ds_read_b64_tr_b16 a[8:9], v3 offset:2048\n"
      "ds_read_b64_tr_b16 a[10:11], v3 offset:2560\n"
      "v_lshlrev_b32 v228, 16, v114\n"
      "v_and_b32 v229, 0xffff0000, v114\n"
      "v_pk_fma_f32 v[52:53], v[36:37], v[228:229], v[52:53]\n"
      "v_lshlrev_b32 v54, 16, v123\n"
      "s_waitcnt vmcnt(12)\n"
      "s_add_u32 m0, s39, 32768\n"
      "s_add_u32 s40, s40, 0x8000\n"
      "s_and_b32 s40, s40, 0xfffff\n"
      "buffer_load_dwordx4 v1, s[20:23], s40 offen  lds\n"
      "ds_read_b64_tr_b16 a[20:21], v8 offset:4096\n"
      "ds_read_b64_tr_b16 a[22:23], v8 offset:4608\n"
      "ds_read_b64_tr_b16 a[16:17], v3 offset:4096\n"
      "ds_read_b64_tr_b16 a[18:19], v3 offset:4608\n"
      "v_and_b32 v55, 0xffff0000, v123\n"
      "v_lshlrev_b32 v230, 16, v115\n"
      "v_and_b32 v231, 0xffff0000, v115\n"
      "v_pk_fma_f32 v[54:55], v[38:39], v[230:231], v[54:55]\n"
      "s_waitcnt vmcnt(12)\n"
      "s_add_u32 m0, s39, 34816\n"
      "s_add_u32 s40, s40, 0x8000\n"
      "s_and_b32 s40, s40, 0xfffff\n"
      "buffer_load_dwordx4 v1, s[20:23], s40 offen  lds\n"
      "s_waitcnt lgkmcnt(8)\n"
      "ds_read_b64_tr_b16 a[28:29], v8 offset:6144\n"
      "ds_read_b64_tr_b16 a[30:31], v8 offset:6656\n"
      "ds_read_b64_tr_b16 a[24:25], v3 offset:6144\n"
      "ds_read_b64_tr_b16 a[26:27], v3 offset:6656\n"
      "v_lshlrev_b32 v56, 16, v124\n"
      "v_and_b32 v57, 0xffff0000, v124\n"
      "v_lshlrev_b32 v232, 16, v116\n"
      "v_and_b32 v233, 0xffff0000, v116\n"
      "s_waitcnt vmcnt(12)\n"
      "s_add_u32 m0, s39, 36864\n"
      "s_add_u32 s40, s40, 0x8000\n"
      "s_and_b32 s40, s40, 0xfffff\n"
      "buffer_load_dwordx4 v1, s[20:23], s40 offen  lds\n"
      "s_waitcnt lgkmcnt(0)\n"
      "s_barrier\n"
      "ds_read_b64_tr_b16 a[36:37], v8 offset:8192\n"
      "ds_read_b64_tr_b16 a[38:39], v8 offset:8704\n"
      "ds_read_b64_tr_b16 a[32:33], v3 offset:8192\n"
      "ds_read_b64_tr_b16 a[34:35], v3 offset:8704\n"
      "v_pk_fma_f32 v[56:57], v[40:41], v[232:233], v[56:57]\n"
      "v_lshlrev_b32 v58, 16, v125\n"
      "v_and_b32 v59, 0xffff0000, v125\n"
      "v_lshlrev_b32 v234, 16, v117\n"
      "s_waitcnt vmcnt(12)\n"
      "s_add_u32 m0, s39, 38912\n"
      "s_add_u32 s40, s40, 0x8000\n"
      "s_and_b32 s40, s40, 0xfffff\n"
      "buffer_load_dwordx4 v1, s[20:23], s40 offen  lds\n"
      "ds_read_b64_tr_b16 a[44:45], v8 offset:10240\n"
      "ds_read_b64_tr_b16 a[46:47], v8 offset:10752\n"
      "ds_read_b64_tr_b16 a[40:41], v3 offset:10240\n"
      "ds_read_b64_tr_b16 a[42:43], v3 offset:10752\n"
      "v_and_b32 v235, 0xffff0000, v117\n"
      "v_pk_fma_f32 v[58:59], v[42:43], v[234:235], v[58:59]\n"
      "v_lshlrev_b32 v60, 16, v126\n"
      "v_and_b32 v61, 0xffff0000, v126\n"
      "s_waitcnt vmcnt(12)\n"
      "s_add_u32 m0, s39, 40960\n"
      "s_add_u32 s40, s40, 0x8000\n"
      "s_and_b32 s40, s40, 0xfffff\n"
      "buffer_load_dwordx4 v1, s[20:23], s40 offen  lds\n"
      "ds_read_b64_tr_b16 a[52:53], v8 offset:12288\n"
      "ds_read_b64_tr_b16 a[54:55], v8 offset:12800\n"
      "ds_read_b64_tr_b16 a[48:49], v3 offset:12288\n"
      "ds_read_b64_tr_b16 a[50:51], v3 offset:12800\n"
      "v_lshlrev_b32 v236, 16, v118\n"
      "v_and_b32 v237, 0xffff0000, v118\n"
      "v_pk_fma_f32 v[60:61], v[44:45], v[236:237], v[60:61]\n"
      "v_lshlrev_b32 v62, 16, v127\n"
      "s_waitcnt vmcnt(12)\n"
      "s_add_u32 m0, s39, 43008\n"
      "s_add_u32 s40, s40, 0x8000\n"
      "s_and_b32 s40, s40, 0xfffff\n"
      "buffer_load_dwordx4 v1, s[20:23], s40 offen  lds\n"
      "s_waitcnt lgkmcnt(8)\n"
      "ds_read_b64_tr_b16 a[60:61], v8 offset:14336\n"
      "ds_read_b64_tr_b16 a[62:63], v8 offset:14848\n"
      "ds_read_b64_tr_b16 a[56:57], v3 offset:14336\n"
      "ds_read_b64_tr_b16 a[58:59], v3 offset:14848\n"
      "v_and_b32 v63, 0xffff0000, v127\n"
      "v_lshlrev_b32 v238, 16, v119\n"
      "v_and_b32 v239, 0xffff0000, v119\n"
      "v_pk_fma_f32 v[62:63], v[46:47], v[238:239], v[62:63]\n"
      "s_waitcnt vmcnt(12)\n"
      "s_add_u32 m0, s39, 45056\n"
      "s_add_u32 s40, s40, 0x8000\n"
      "s_and_b32 s40, s40, 0xfffff\n"
      "buffer_load_dwordx4 v1, s[20:23], s40 offen  lds\n"
      "s_waitcnt lgkmcnt(0)\n"
      "s_barrier\n"
      "ds_read_b64_tr_b16 a[68:69], v8 offset:16384\n"
      "ds_read_b64_tr_b16 a[70:71], v8 offset:16896\n"
      "ds_read_b64_tr_b16 a[64:65], v3 offset:16384\n"
      "ds_read_b64_tr_b16 a[66:67], v3 offset:16896\n"
      "s_waitcnt vmcnt(12)\n"
      "s_add_u32 m0, s39, 47104\n"
      "s_add_u32 s40, s40, 0x8000\n"
      "s_and_b32 s40, s40, 0xfffff\n"
      "buffer_load_dwordx4 v1, s[20:23], s40 offen  lds\n"
      "ds_read_b64_tr_b16 a[76:77], v8 offset:18432\n"
      "ds_read_b64_tr_b16 a[78:79], v8 offset:18944\n"
      "ds_read_b64_tr_b16 a[72:73], v3 offset:18432\n"
      "ds_read_b64_tr_b16 a[74:75], v3 offset:18944\n"
      "s_waitcnt vmcnt(12)\n"
      "s_add_u32 m0, s39, 49152\n"
      "s_add_u32 s40, s40, 0x8000\n"
      "s_and_b32 s40, s40, 0xfffff\n"
      "buffer_load_dwordx4 v1, s[20:23], s40 offen  lds\n"
      "ds_read_b64_tr_b16 a[84:85], v8 offset:20480\n"
      "ds_read_b64_tr_b16 a[86:87], v8 offset:20992\n"
      "ds_read_b64_tr_b16 a[80:81], v3 offset:20480\n"
      "ds_read_b64_tr_b16 a[82:83], v3 offset:20992\n"
      "s_waitcnt vmcnt(10)\n"
      "s_add_u32 m0, s39, 51200\n"
      "s_add_u32 s40, s40, 0x8000\n"
      "s_and_b32 s40, s40, 0xfffff\n"
      "buffer_load_dwordx4 v1, s[20:23], s40 offen  lds\n"
      "s_waitcnt lgkmcnt(8)\n"
      "ds_read_b64_tr_b16 a[92:93], v8 offset:22528\n"
      "ds_read_b64_tr_b16 a[94:95], v8 offset:23040\n"
      "ds_read_b64_tr_b16 a[88:89], v3 offset:22528\n"
      "ds_read_b64_tr_b16 a[90:91], v3 offset:23040\n"
      "s_waitcnt vmcnt(10)\n"
      "s_add_u32 m0, s39, 53248\n"
      "s_add_u32 s40, s40, 0x8000\n"
      "s_and_b32 s40, s40, 0xfffff\n"
      "buffer_load_dwordx4 v1, s[20:23], s40 offen  lds\n"
      "s_waitcnt lgkmcnt(0)\n"
      "s_barrier\n"
      "ds_read_b64_tr_b16 a[100:101], v8 offset:24576\n"
      "ds_read_b64_tr_b16 a[102:103], v8 offset:25088\n"
      "ds_read_b64_tr_b16 a[96:97], v3 offset:24576\n"
      "ds_read_b64_tr_b16 a[98:99], v3 offset:25088\n"
      "s_waitcnt vmcnt(10)\n"
      "s_add_u32 m0, s39, 55296\n"
      "s_add_u32 s40, s40, 0x8000\n"
      "s_and_b32 s40, s40, 0xfffff\n"
      "buffer_load_dwordx4 v1, s[20:23], s40 offen  lds\n"
      "ds_read_b64_tr_b16 a[108:109], v8 offset:26624\n"
      "ds_read_b64_tr_b16 a[110:111], v8 offset:27136\n"
      "ds_read_b64_tr_b16 a[104:105], v3 offset:26624\n"
      "ds_read_b64_tr_b16 a[106:107], v3 offset:27136\n"
      "s_waitcnt vmcnt(10)\n"
      "s_add_u32 m0, s39, 57344\n"
      "s_add_u32 s40, s40, 0x8000\n"
      "s_and_b32 s40, s40, 0xfffff\n"
      "buffer_load_dwordx4 v1, s[20:23], s40 offen  lds\n"
      "ds_read_b64_tr_b16 a[116:117], v8 offset:28672\n"
      "ds_read_b64_tr_b16 a[118:119], v8 offset:29184\n"
      "ds_read_b64_tr_b16 a[112:113], v3 offset:28672\n"
      "ds_read_b64_tr_b16 a[114:115], v3 offset:29184\n"
      "s_waitcnt vmcnt(10)\n"
      "s_add_u32 m0, s39, 59392\n"
      "s_add_u32 s40, s40, 0x8000\n"
      "s_and_b32 s40, s40, 0xfffff\n"
      "buffer_load_dwordx4 v1, s[20:23], s40 offen  lds\n"
      "s_waitcnt lgkmcnt(8)\n"
      "ds_read_b64_tr_b16 a[124:125], v8 offset:30720\n"
      "ds_read_b64_tr_b16 a[126:127], v8 offset:31232\n"
      "ds_read_b64_tr_b16 a[120:121], v3 offset:30720\n"
      "ds_read_b64_tr_b16 a[122:123], v3 offset:31232\n"
      "s_waitcnt vmcnt(10)\n"
      "s_add_u32 m0, s39, 61440\n"
      "s_add_u32 s40, s40, 0x8000\n"
      "s_and_b32 s40, s40, 0xfffff\n"
      "buffer_load_dwordx4 v1, s[20:23], s40 offen  lds\n"
      "s_waitcnt lgkmcnt(0)\n"
      "s_barrier\n"
      "ds_read_b64_tr_b16 a[132:133], v8 offset:32768\n"
      "ds_read_b64_tr_b16 a[134:135], v8 offset:33280\n"
      "ds_read_b64_tr_b16 a[128:129], v3 offset:32768\n"
      "ds_read_b64_tr_b16 a[130:131], v3 offset:33280\n"
      "s_waitcnt vmcnt(10)\n"
      "s_add_u32 m0, s39, 63488\n"
      "s_add_u32 s40, s40, 0x8000\n"
      "s_and_b32 s40, s40, 0xfffff\n"
      "buffer_load_dwordx4 v1, s[20:23], s40 offen  lds\n"
      "ds_read_b64_tr_b16 a[140:141], v8 offset:34816\n"
      "ds_read_b64_tr_b16 a[142:143], v8 offset:35328\n"
      "ds_read_b64_tr_b16 a[136:137], v3 offset:34816\n"
      "ds_read_b64_tr_b16 a[138:139], v3 offset:35328\n"
      "s_waitcnt vmcnt(10)\n"
      "ds_read_b64_tr_b16 a[148:149], v8 offset:36864\n"
      "ds_read_b64_tr_b16 a[150:151], v8 offset:37376\n"
      "ds_read_b64_tr_b16 a[144:145], v3 offset:36864\n"
      "ds_read_b64_tr_b16 a[146:147], v3 offset:37376\n"
      "s_waitcnt vmcnt(9)\n"
      "s_waitcnt lgkmcnt(8)\n"
      "ds_read_b64_tr_b16 a[156:157], v8 offset:38912\n"
      "ds_read_b64_tr_b16 a[158:159], v8 offset:39424\n"
      "ds_read_b64_tr_b16 a[152:153], v3 offset:38912\n"
      "ds_read_b64_tr_b16 a[154:155], v3 offset:39424\n"
      "s_waitcnt vmcnt(8)\n"
      "s_waitcnt lgkmcnt(0)\n"
      "s_barrier\n"
      "ds_read_b64_tr_b16 a[164:165], v8 offset:40960\n"
      "ds_read_b64_tr_b16 a[166:167], v8 offset:41472\n"
      "ds_read_b64_tr_b16 a[160:161], v3 offset:40960\n"
      "ds_read_b64_tr_b16 a[162:163], v3 offset:41472\n"
      "s_waitcnt vmcnt(7)\n"
      "ds_read_b64_tr_b16 a[172:173], v8 offset:43008\n"
      "ds_read_b64_tr_b16 a[174:175], v8 offset:43520\n"
      "ds_read_b64_tr_b16 a[168:169], v3 offset:43008\n"
      "ds_read_b64_tr_b16 a[170:171], v3 offset:43520\n"
      "s_waitcnt vmcnt(6)\n"
      "s_lshl_b32 s41, s50, 10\n"
      "v_add_u32 v13, 0x200000, v13\n"
      "buffer_load_dwordx4 v[128:131], v13, s[64:67], s41 offen sc1\n"
      "s_add_u32 s41, s41, 0x400\n"
      "s_and_b32 s41, s41, 0x7fff\n"
      "buffer_load_dwordx4 v[132:135], v13, s[64:67], s41 offen sc1\n"
      "s_add_u32 s41, s41, 0x400\n"
      "s_and_b32 s41, s41, 0x7fff\n"
      "buffer_load_dwordx4 v[136:139], v13, s[64:67], s41 offen sc1\n"
      "s_add_u32 s41, s41, 0x400\n"
      "s_and_b32 s41, s41, 0x7fff\n"
      "buffer_load_dwordx4 v[140:143], v13, s[64:67], s41 offen sc1\n"
      "ds_read_b64_tr_b16 a[180:181], v8 offset:45056\n"
      "ds_read_b64_tr_b16 a[182:183], v8 offset:45568\n"
      "ds_read_b64_tr_b16 a[176:177], v3 offset:45056\n"
      "ds_read_b64_tr_b16 a[178:179], v3 offset:45568\n"
      "s_waitcnt vmcnt(9)\n"
      "s_add_u32 s41, s41, 0x400\n"
      "s_and_b32 s41, s41, 0x7fff\n"
      "buffer_load_dwordx4 v[144:147], v13, s[64:67], s41 offen sc1\n"
      "s_waitcnt lgkmcnt(8)\n"
      "ds_read_b64_tr_b16 a[188:189], v8 offset:47104\n"
      "ds_read_b64_tr_b16 a[190:191], v8 offset:47616\n"
      "ds_read_b64_tr_b16 a[184:185], v3 offset:47104\n"
      "ds_read_b64_tr_b16 a[186:187], v3 offset:47616\n"
      "s_waitcnt vmcnt(9)\n"
      "s_add_u32 s41, s41, 0x400\n"
      "s_and_b32 s41, s41, 0x7fff\n"
      "buffer_load_dwordx4 v[148:151], v13, s[64:67], s41 offen sc1\n"
      "s_waitcnt lgkmcnt(0)\n"
      "s_barrier\n"
      "ds_read_b64_tr_b16 a[196:197], v8 offset:49152\n"
      "ds_read_b64_tr_b16 a[198:199], v8 offset:49664\n"
      "ds_read_b64_tr_b16 a[192:193], v3 offset:49152\n"
      "ds_read_b64_tr_b16 a[194:195], v3 offset:49664\n"
      "s_waitcnt vmcnt(9)\n"
      "s_add_u32 s41, s41, 0x400\n"
      "s_and_b32 s41, s41, 0x7fff\n"
      "buffer_load_dwordx4 v[152:155], v13, s[64:67], s41 offen sc1\n"
      "ds_read_b64_tr_b16 a[204:205], v8 offset:51200\n"
      "ds_read_b64_tr_b16 a[206:207], v8 offset:51712\n"
      "ds_read_b64_tr_b16 a[200:201], v3 offset:51200\n"
      "ds_read_b64_tr_b16 a[202:203], v3 offset:51712\n"
      "s_waitcnt vmcnt(9)\n"
      "s_add_u32 s41, s41, 0x400\n"
      "s_and_b32 s41, s41, 0x7fff\n"
      "buffer_load_dwordx4 v[156:159], v13, s[64:67], s41 offen sc1\n"
      "ds_read_b64_tr_b16 a[212:213], v8 offset:53248\n"
      "ds_read_b64_tr_b16 a[214:215], v8 offset:53760\n"
      "ds_read_b64_tr_b16 a[208:209], v3 offset:53248\n"
      "ds_read_b64_tr_b16 a[210:211], v3 offset:53760\n"
      "s_waitcnt vmcnt(9)\n"
      "s_add_u32 s41, s41, 0x400\n"
      "s_and_b32 s41, s41, 0x7fff\n"
      "buffer_load_dwordx4 v[160:163], v13, s[64:67], s41 offen sc1\n"
      "s_waitcnt lgkmcnt(8)\n"
      "ds_read_b64_tr_b16 a[220:221], v8 offset:55296\n"
      "ds_read_b64_tr_b16 a[222:223], v8 offset:55808\n"
      "ds_read_b64_tr_b16 a[216:217], v3 offset:55296\n"
      "ds_read_b64_tr_b16 a[218:219], v3 offset:55808\n"
      "s_waitcnt vmcnt(9)\n"
      "s_add_u32 s41, s41, 0x400\n"
      "s_and_b32 s41, s41, 0x7fff\n"
      "buffer_load_dwordx4 v[164:167], v13, s[64:67], s41 offen sc1\n"
      "s_waitcnt lgkmcnt(0)\n"
      "s_barrier\n"
      ".p2align 5\n"
      "s_waitcnt vmcnt(9)\n"
      "s_add_u32 s41, s41, 0x400\n"
      "s_and_b32 s41, s41, 0x7fff\n"
      "buffer_load_dwordx4 v[168:171], v13, s[64:67], s41 offen sc1\n"
      "v_max3_u32 v22, v128, v129, v130\n"
      "v_max_u32 v22, v22, v131\n"
      "v_cmp_eq_u32 vcc, -1, v22\n"
      "s_cbranch_vccnz L_f2_0\n"
      "L_k2_0:\n"
      "s_waitcnt vmcnt(9)\n"
      "v_mfma_f32_32x32x16_bf16 v[80:95], a[4:7], v[128:131], 0\n"
      "v_max3_u32 v22, v132, v133, v134\n"
      "s_add_u32 s41, s41, 0x400\n"
      "s_and_b32 s41, s41, 0x7fff\n"
      "v_max_u32 v22, v22, v135\n"
      "v_mfma_f32_32x32x16_bf16 v[64:79], a[0:3], v[128:131], 0\n"
      "v_cmp_eq_u32 vcc, -1, v22\n"
      "buffer_load_dwordx4 v[172:175], v13, s[64:67], s41 offen sc1\n"
      "s_cbranch_vccnz L_f2_1\n"
      "L_k2_1:\n"
      "s_waitcnt vmcnt(9)\n"
      "v_mfma_f32_32x32x16_bf16 v[80:95], a[12:15], v[132:135], v[80:95]\n"
      "v_max3_u32 v22, v136, v137, v138\n"
      "s_add_u32 s41, s41, 0x400\n"
      "s_and_b32 s41, s41, 0x7fff\n"
      "v_max_u32 v22, v22, v139\n"
      "v_mfma_f32_32x32x16_bf16 v[64:79], a[8:11], v[132:135], v[64:79]\n"
      "v_cmp_eq_u32 vcc, -1, v22\n"
      "buffer_load_dwordx4 v[176:179], v13, s[64:67], s41 offen sc1\n"
      "s_cbranch_vccnz L_f2_2\n"
      "L_k2_2:\n"
      "s_waitcnt vmcnt(9)\n"
      "v_mfma_f32_32x32x16_bf16 v[80:95], a[20:23], v[136:139], v[80:95]\n"
      "v_max3_u32 v22, v140, v141, v142\n"
      "s_add_u32 s41, s41, 0x400\n"
      "s_and_b32 s41, s41, 0x7fff\n"
      "v_max_u32 v22, v22, v143\n"
      "v_mfma_f32_32x32x16_bf16 v[64:79], a[16:19], v[136:139], v[64:79]\n"
      "v_cmp_eq_u32 vcc, -1, v22\n"
      "buffer_load_dwordx4 v[180:183], v13, s[64:67], s41 offen sc1\n"
      "s_cbranch_vccnz L_f2_3\n"
      "L_k2_3:\n"
      "s_waitcnt vmcnt(9)\n"
      "v_mfma_f32_32x32x16_bf16 v[80:95], a[28:31], v[140:143], v[80:95]\n"
      "v_max3_u32 v22, v144, v145, v146\n"
      "s_add_u32 s41, s41, 0x400\n"
      "s_and_b32 s41, s41, 0x7fff\n"
      "v_max_u32 v22, v22, v147\n"
      "v_mfma_f32_32x32x16_bf16 v[64:79], a[24:27], v[140:143], v[64:79]\n"
      "v_cmp_eq_u32 vcc, -1, v22\n"
      "buffer_load_dwordx4 v[184:187], v13, s[64:67], s41 offen sc1\n"
      "s_cbranch_vccnz L_f2_4\n"
      "L_k2_4:\n"
      "s_waitcnt vmcnt(9)\n"
      "v_mfma_f32_32x32x16_bf16 v[80:95], a[36:39], v[144:147], v[80:95]\n"
      "v_max3_u32 v22, v148, v149, v150\n"
      "s_add_u32 s41, s41, 0x400\n"
      "s_and_b32 s41, s41, 0x7fff\n"
      "v_max_u32 v22, v22, v151\n"
      "v_mfma_f32_32x32x16_bf16 v[64:79], a[32:35], v[144:147], v[64:79]\n"
      "v_cmp_eq_u32 vcc, -1, v22\n"
      "buffer_load_dwordx4 v[188:191], v13, s[64:67], s41 offen sc1\n"
      "s_cbranch_vccnz L_f2_5\n"
      "L_k2_5:\n"
      "s_waitcnt vmcnt(9)\n"
      "v_mfma_f32_32x32x16_bf16 v[80:95], a[44:47], v[148:151], v[80:95]\n"
      "v_max3_u32 v22, v152, v153, v154\n"
      "s_add_u32 s41, s41, 0x400\n"
      "s_and_b32 s41, s41, 0x7fff\n"
      "v_max_u32 v22, v22, v155\n"
      "v_mfma_f32_32x32x16_bf16 v[64:79], a[40:43], v[148:151], v[64:79]\n"
      "v_cmp_eq_u32 vcc, -1, v22\n"
      "buffer_load_dwordx4 v[192:195], v13, s[64:67], s41 offen sc1\n"
      "s_cbranch_vccnz L_f2_6\n"
      "L_k2_6:\n"
      "s_waitcnt vmcnt(9)\n"
      "v_mfma_f32_32x32x16_bf16 v[80:95], a[52:55], v[152:155], v[80:95]\n"
      "v_max3_u32 v22, v156, v157, v158\n"
      "s_add_u32 s41, s41, 0x400\n"
      "s_and_b32 s41, s41, 0x7fff\n"
      "v_max_u32 v22, v22, v159\n"
      "v_mfma_f32_32x32x16_bf16 v[64:79], a[48:51], v[152:155], v[64:79]\n"
      "v_cmp_eq_u32 vcc, -1, v22\n"
      "buffer_load_dwordx4 v[196:199], v13, s[64:67], s41 offen sc1\n"
      "s_cbranch_vccnz L_f2_7\n"
      "L_k2_7:\n"
      "s_waitcnt vmcnt(9)\n"
      "v_mfma_f32_32x32x16_bf16 v[80:95], a[60:63], v[156:159], v[80:95]\n"
      "v_max3_u32 v22, v160, v161, v162\n"
      "s_add_u32 s41, s41, 0x400\n"
      "s_and_b32 s41, s41, 0x7fff\n"
      "v_max_u32 v22, v22, v163\n"
      "v_mfma_f32_32x32x16_bf16 v[64:79], a[56:59], v[156:159], v[64:79]\n"
      "v_cmp_eq_u32 vcc, -1, v22\n"
      "buffer_load_dwordx4 v[200:203], v13, s[64:67], s41 offen sc1\n"
      "s_cbranch_vccnz L_f2_8\n"
      "L_k2_8:\n"
      "s_waitcnt vmcnt(9)\n"
      "v_mfma_f32_32x32x16_bf16 v[80:95], a[68:71], v[160:163], v[80:95]\n"
      "v_max3_u32 v22, v164, v165, v166\n"
      "s_add_u32 s41, s41, 0x400\n"
      "s_and_b32 s41, s41, 0x7fff\n"
      "v_max_u32 v22, v22, v167\n"
      "v_mfma_f32_32x32x16_bf16 v[64:79], a[64:67], v[160:163], v[64:79]\n"
      "v_cmp_eq_u32 vcc, -1, v22\n"
      "buffer_load_dwordx4 v[204:207], v13, s[64:67], s41 offen sc1\n"
      "s_cbranch_vccnz L_f2_9\n"
      "L_k2_9:\n"
      "s_waitcnt vmcnt(9)\n"
      "v_mfma_f32_32x32x16_bf16 v[80:95], a[76:79], v[164:167], v[80:95]\n"
      "v_max3_u32 v22, v168, v169, v170\n"
      "s_add_u32 s41, s41, 0x400\n"
      "s_and_b32 s41, s41, 0x7fff\n"
      "v_max_u32 v22, v22, v171\n"
      "v_mfma_f32_32x32x16_bf16 v[64:79], a[72:75], v[164:167], v[64:79]\n"
      "v_cmp_eq_u32 vcc, -1, v22\n"
      "buffer_load_dwordx4 v[208:211], v13, s[64:67], s41 offen sc1\n"
      "s_cbranch_vccnz L_f2_10\n"
      "L_k2_10:\n"
      "s_waitcnt vmcnt(9)\n"
      "v_mfma_f32_32x32x16_bf16 v[80:95], a[84:87], v[168:171], v[80:95]\n"
      "v_max3_u32 v22, v172, v173, v174\n"
      "s_add_u32 s41, s41, 0x400\n"
      "s_and_b32 s41, s41, 0x7fff\n"
      "v_max_u32 v22, v22, v175\n"
      "v_mfma_f32_32x32x16_bf16 v[64:79], a[80:83], v[168:171], v[64:79]\n"
      "v_cmp_eq_u32 vcc, -1, v22\n"
      "buffer_load_dwordx4 v[212:215], v13, s[64:67], s41 offen sc1\n"
      "s_cbranch_vccnz L_f2_11\n"
      "L_k2_11:\n"
      "v_mfma_f32_32x32x16_bf16 v[80:95], a[92:95], v[172:175], v[80:95]\n"
      "v_mfma_f32_32x32x16_bf16 v[64:79], a[88:91], v[172:175], v[64:79]\n"
      "s_waitcnt vmcnt(9)\n"
      "v_mfma_f32_32x32x16_bf16 v[80:95], a[100:103], v[176:179], v[80:95]\n"
      "s_add_u32 s41, s41, 0x400\n"
      "s_and_b32 s41, s41, 0x7fff\n"
      "v_mfma_f32_32x32x16_bf16 v[64:79], a[96:99], v[176:179], v[64:79]\n"
      "buffer_load_dwordx4 v[216:219], v13, s[64:67], s41 offen sc1\n"
      "s_waitcnt vmcnt(9)\n"
      "v_mfma_f32_32x32x16_bf16 v[80:95], a[108:111], v[180:183], v[80:95]\n"
      "s_add_u32 s41, s41, 0x400\n"
      "s_and_b32 s41, s41, 0x7fff\n"
      "ds_read_b64_tr_b16 a[228:229], v8 offset:57344\n"
      "ds_read_b64_tr_b16 a[230:231], v8 offset:57856\n"
      "v_mfma_f32_32x32x16_bf16 v[64:79], a[104:107], v[180:183], v[64:79]\n"
      "ds_read_b64_tr_b16 a[224:225], v3 offset:57344\n"
      "ds_read_b64_tr_b16 a[226:227], v3 offset:57856\n"
      "buffer_load_dwordx4 v[220:223], v13, s[64:67], s41 offen sc1\n"
      "s_waitcnt vmcnt(9)\n"
      "v_mfma_f32_32x32x16_bf16 v[80:95], a[116:119], v[184:187], v[80:95]\n"
      "s_add_u32 s41, s41, 0x400\n"
      "s_and_b32 s41, s41, 0x7fff\n"
      "ds_read_b64_tr_b16 a[236:237], v8 offset:59392\n"
      "ds_read_b64_tr_b16 a[238:239], v8 offset:59904\n"
      "v_mfma_f32_32x32x16_bf16 v[64:79], a[112:115], v[184:187], v[64:79]\n"
      "ds_read_b64_tr_b16 a[232:233], v3 offset:59392\n"
      "ds_read_b64_tr_b16 a[234:235], v3 offset:59904\n"
      "buffer_load_dwordx4 v[224:227], v13, s[64:67], s41 offen sc1\n"
      "s_waitcnt vmcnt(9)\n"
      "v_mfma_f32_32x32x16_bf16 v[80:95], a[124:127], v[188:191], v[80:95]\n"
      "s_add_u32 s41, s41, 0x400\n"
      "s_and_b32 s41, s41, 0x7fff\n"
      "ds_read_b64_tr_b16 a[244:245], v8 offset:61440\n"
      "ds_read_b64_tr_b16 a[246:247], v8 offset:61952\n"
      "v_mfma_f32_32x32x16_bf16 v[64:79], a[120:123], v[188:191], v[64:79]\n"
      "ds_read_b64_tr_b16 a[240:241], v3 offset:61440\n"
      "ds_read_b64_tr_b16 a[242:243], v3 offset:61952\n"
      "buffer_load_dwordx4 v[228:231], v13, s[64:67], s41 offen sc1\n"
      "s_waitcnt vmcnt(9)\n"
      "v_mfma_f32_32x32x16_bf16 v[80:95], a[132:135], v[192:195], v[80:95]\n"
      "s_add_u32 s41, s41, 0x400\n"
      "s_and_b32 s41, s41, 0x7fff\n"
      "s_waitcnt lgkmcnt(8)\n"
      "ds_read_b64_tr_b16 a[252:253], v8 offset:63488\n"
      "ds_read_b64_tr_b16 a[254:255], v8 offset:64000\n"
      "v_mfma_f32_32x32x16_bf16 v[64:79], a[128:131], v[192:195], v[64:79]\n"
      "ds_read_b64_tr_b16 a[248:249], v3 offset:63488\n"
      "ds_read_b64_tr_b16 a[250:251], v3 offset:64000\n"
      "buffer_load_dwordx4 v[232:235], v13, s[64:67], s41 offen sc1\n"
      "s_waitcnt vmcnt(9)\n"
      "v_mfma_f32_32x32x16_bf16 v[80:95], a[140:143], v[196:199], v[80:95]\n"
      "s_add_u32 s41, s41, 0x400\n"
      "s_and_b32 s41, s41, 0x7fff\n"
      "v_mfma_f32_32x32x16_bf16 v[64:79], a[136:139], v[196:199], v[64:79]\n"
      "buffer_load_dwordx4 v[236:239], v13, s[64:67], s41 offen sc1\n"
      "s_waitcnt vmcnt(9)\n"
      "v_mfma_f32_32x32x16_bf16 v[80:95], a[148:151], v[200:203], v[80:95]\n"
      "s_add_u32 s41, s41, 0x400\n"
      "s_and_b32 s41, s41, 0x7fff\n"
      "v_mfma_f32_32x32x16_bf16 v[64:79], a[144:147], v[200:203], v[64:79]\n"
      "buffer_load_dwordx4 v[240:243], v13, s[64:67], s41 offen sc1\n"
      "s_waitcnt vmcnt(9)\n"
      "v_mfma_f32_32x32x16_bf16 v[80:95], a[156:159], v[204:207], v[80:95]\n"
      "s_add_u32 s41, s41, 0x400\n"
      "s_and_b32 s41, s41, 0x7fff\n"
      "v_mfma_f32_32x32x16_bf16 v[64:79], a[152:155], v[204:207], v[64:79]\n"
      "buffer_load_dwordx4 v[244:247], v13, s[64:67], s41 offen sc1\n"
      "s_waitcnt vmcnt(9)\n"
      "v_mfma_f32_32x32x16_bf16 v[80:95], a[164:167], v[208:211], v[80:95]\n"
      "s_add_u32 s41, s41, 0x400\n"
      "s_and_b32 s41, s41, 0x7fff\n"
      "v_mfma_f32_32x32x16_bf16 v[64:79], a[160:163], v[208:211], v[64:79]\n"
      "buffer_load_dwordx4 v[248:251], v13, s[64:67], s41 offen sc1\n"
      "s_waitcnt vmcnt(9)\n"
      "v_mfma_f32_32x32x16_bf16 v[80:95], a[172:175], v[212:215], v[80:95]\n"
      "s_add_u32 s41, s41, 0x400\n"
      "s_and_b32 s41, s41, 0x7fff\n"
      "v_mfma_f32_32x32x16_bf16 v[64:79], a[168:171], v[212:215], v[64:79]\n"
      "buffer_load_dwordx4 v[252:255], v13, s[64:67], s41 offen sc1\n"
      "s_waitcnt vmcnt(9)\n"
      "v_mfma_f32_32x32x16_bf16 v[80:95], a[180:183], v[216:219], v[80:95]\n"
      "v_mfma_f32_32x32x16_bf16 v[64:79], a[176:179], v[216:219], v[64:79]\n"
      "s_waitcnt vmcnt(8)\n"
      "v_mfma_f32_32x32x16_bf16 v[80:95], a[188:191], v[220:223], v[80:95]\n"
      "v_mfma_f32_32x32x16_bf16 v[64:79], a[184:187], v[220:223], v[64:79]\n"
      "s_waitcnt vmcnt(7)\n"
      "v_mfma_f32_32x32x16_bf16 v[80:95], a[196:199], v[224:227], v[80:95]\n"
      "v_mfma_f32_32x32x16_bf16 v[64:79], a[192:195], v[224:227], v[64:79]\n"
      "s_waitcnt vmcnt(6)\n"
      "v_mfma_f32_32x32x16_bf16 v[80:95], a[204:207], v[228:231], v[80:95]\n"
      "v_mfma_f32_32x32x16_bf16 v[64:79], a[200:203], v[228:231], v[64:79]\n"
      "s_waitcnt vmcnt(5)\n"
      "v_mfma_f32_32x32x16_bf16 v[80:95], a[212:215], v[232:235], v[80:95]\n"
      "v_mfma_f32_32x32x16_bf16 v[64:79], a[208:211], v[232:235], v[64:79]\n"
      "s_waitcnt vmcnt(4)\n"
      "v_mfma_f32_32x32x16_bf16 v[80:95], a[220:223], v[236:239], v[80:95]\n"
      "v_mfma_f32_32x32x16_bf16 v[64:79], a[216:219], v[236:239], v[64:79]\n"
      "s_waitcnt vmcnt(3)\n"
      "s_waitcnt lgkmcnt(0)\n"
      "v_mfma_f32_32x32x16_bf16 v[80:95], a[228:231], v[240:243], v[80:95]\n"
      "v_mfma_f32_32x32x16_bf16 v[64:79], a[224:227], v[240:243], v[64:79]\n"
      "s_waitcnt vmcnt(2)\n"
      "v_mfma_f32_32x32x16_bf16 v[80:95], a[236:239], v[244:247], v[80:95]\n"
      "v_mfma_f32_32x32x16_bf16 v[64:79], a[232:235], v[244:247], v[64:79]\n"
      "s_waitcnt vmcnt(1)\n"
      "v_mfma_f32_32x32x16_bf16 v[80:95], a[244:247], v[248:251], v[80:95]\n"
      "v_mfma_f32_32x32x16_bf16 v[64:79], a[240:243], v[248:251], v[64:79]\n"
      "s_waitcnt vmcnt(0)\n"
      "v_mfma_f32_32x32x16_bf16 v[80:95], a[252:255], v[252:255], v[80:95]\n"
      "v_mfma_f32_32x32x16_bf16 v[64:79], a[248:251], v[252:255], v[64:79]\n"
      "s_nop 11\n"
      "v_cmp_u_f32 vcc, v80, v80\n"
      "s_cbranch_vccnz L_nan2\n"
      "L_nan2_x:\n"
      "ds_write_b128 v16, v[80:83] offset:0\n"
      "ds_write_b128 v16, v[84:87] offset:1024\n"
      "ds_write_b128 v16, v[88:91] offset:2048\n"
      "ds_write_b128 v16, v[92:95] offset:3072\n"
      "v_pk_fma_f32 v[64:65], v[32:33], v[64:65], v[48:49]\n"
      "v_pk_fma_f32 v[66:67], v[34:35], v[66:67], v[50:51]\n"
      "v_pk_fma_f32 v[68:69], v[36:37], v[68:69], v[52:53]\n"
      "v_pk_fma_f32 v[70:71], v[38:39], v[70:71], v[54:55]\n"
      "v_pk_fma_f32 v[72:73], v[40:41], v[72:73], v[56:57]\n"
      "v_pk_fma_f32 v[74:75], v[42:43], v[74:75], v[58:59]\n"
      "v_pk_fma_f32 v[76:77], v[44:45], v[76:77], v[60:61]\n"
      "v_pk_fma_f32 v[78:79], v[46:47], v[78:79], v[62:63]\n"
      "s_waitcnt lgkmcnt(0)\n"
      "s_barrier\n"
      "ds_read_b128 v[128:131], v17 offset:0\n"
      "ds_read_b128 v[132:135], v17 offset:1024\n"
      "ds_read_b128 v[136:139], v17 offset:2048\n"
      "ds_read_b128 v[140:143], v17 offset:3072\n"
      "s_waitcnt lgkmcnt(3)\n"
      "v_pk_fma_f32 v[64:65], v[32:33], v[128:129], v[64:65]\n"
      "v_pk_fma_f32 v[66:67], v[34:35], v[130:131], v[66:67]\n"
      "s_waitcnt lgkmcnt(2)\n"
      "v_pk_fma_f32 v[68:69], v[36:37], v[132:133], v[68:69]\n"
      "v_pk_fma_f32 v[70:71], v[38:39], v[134:135], v[70:71]\n"
      "v_cvt_pk_bf16_f32 v120, v64, v65\n"
      "v_cvt_pk_bf16_f32 v121, v66, v67\n"
      "v_cvt_pk_bf16_f32 v122, v68, v69\n"
      "v_cvt_pk_bf16_f32 v123, v70, v71\n"
      "ds_write_b128 v20, v[120:123]\n"
      "s_waitcnt lgkmcnt(2)\n"
      "v_pk_fma_f32 v[72:73], v[40:41], v[136:137], v[72:73]\n"
      "v_pk_fma_f32 v[74:75], v[42:43], v[138:139], v[74:75]\n"
      "s_waitcnt lgkmcnt(1)\n"
      "v_pk_fma_f32 v[76:77], v[44:45], v[140:141], v[76:77]\n"
      "v_pk_fma_f32 v[78:79], v[46:47], v[142:143], v[78:79]\n"
      "v_cvt_pk_bf16_f32 v124, v72, v73\n"
      "v_cvt_pk_bf16_f32 v125, v74, v75\n"
      "v_cvt_pk_bf16_f32 v126, v76, v77\n"
      "v_cvt_pk_bf16_f32 v127, v78, v79\n"
      "ds_write_b128 v21, v[124:127]\n"
      "s_lshl_b32 s51, s36, 11\n"
      "s_add_u32 s51, s51, 147456\n"
      "v_and_b32 v22, 0x3ff, v16\n"
      "v_add_u32 v22, s51, v22\n"
      "s_waitcnt lgkmcnt(0)\n"
      "s_barrier\n"
      "ds_read_b128 v[160:163], v22 offset:0\n"
      "ds_read_b128 v[164:167], v22 offset:1024\n"
      "s_waitcnt lgkmcnt(1)\n"
      "buffer_store_dwordx4 v[160:163], v18, s[28:31], 0 offen offset:0 sc1\n"
      "s_waitcnt lgkmcnt(0)\n"
      "buffer_store_dwordx4 v[164:167], v19, s[28:31], 0 offen offset:1024 sc1\n"
      "L_fin:\n"
      "s_endpgm\n"
      "L_hot_end:\n"
      "L_f1_0:\n"
      "s_movk_i32 s52, 0\n"
      "s_movk_i32 s54, 9\n"
      "s_call_b64 s[12:13], L_rec1\n"
      "v_max3_u32 v22, v128, v129, v130\n"
      "v_max_u32 v22, v22, v131\n"
      "v_cmp_eq_u32 vcc, -1, v22\n"
      "s_cbranch_vccnz L_f1_0\n"
      "s_branch L_k1_0\n"
      "L_f1_1:\n"
      "s_movk_i32 s52, 1\n"
      "s_movk_i32 s54, 9\n"
      "s_call_b64 s[12:13], L_rec1\n"
      "v_max3_u32 v22, v132, v133, v134\n"
      "v_max_u32 v22, v22, v135\n"
      "v_cmp_eq_u32 vcc, -1, v22\n"
      "s_cbranch_vccnz L_f1_1\n"
      "s_branch L_k1_1\n"
      "L_f1_2:\n"
      "s_movk_i32 s52, 2\n"
      "s_movk_i32 s54, 9\n"
      "s_call_b64 s[12:13], L_rec1\n"
      "v_max3_u32 v22, v136, v137, v138\n"
      "v_max_u32 v22, v22, v139\n"
      "v_cmp_eq_u32 vcc, -1, v22\n"
      "s_cbranch_vccnz L_f1_2\n"
      "s_branch L_k1_2\n"
      "L_f1_3:\n"
      "s_movk_i32 s52, 3\n"
      "s_movk_i32 s54, 9\n"
      "s_call_b64 s[12:13], L_rec1\n"
      "v_max3_u32 v22, v140, v141, v142\n"
      "v_max_u32 v22, v22, v143\n"
      "v_cmp_eq_u32 vcc, -1, v22\n"
      "s_cbranch_vccnz L_f1_3\n"
      "s_branch L_k1_3\n"
      "L_f1_4:\n"
      "s_movk_i32 s52, 4\n"
      "s_movk_i32 s54, 9\n"
      "s_call_b64 s[12:13], L_rec1\n"
      "v_max3_u32 v22, v144, v145, v146\n"
      "v_max_u32 v22, v22, v147\n"
      "v_cmp_eq_u32 vcc, -1, v22\n"
      "s_cbranch_vccnz L_f1_4\n"
      "s_branch L_k1_4\n"
      "L_f1_5:\n"
      "s_movk_i32 s52, 5\n"
      "s_movk_i32 s54, 9\n"
      "s_call_b64 s[12:13], L_rec1\n"
      "v_max3_u32 v22, v148, v149, v150\n"
      "v_max_u32 v22, v22, v151\n"
      "v_cmp_eq_u32 vcc, -1, v22\n"
      "s_cbranch_vccnz L_f1_5\n"
      "s_branch L_k1_5\n"
      "L_f1_6:\n"
      "s_movk_i32 s52, 6\n"
      "s_movk_i32 s54, 9\n"
      "s_call_b64 s[12:13], L_rec1\n"
      "v_max3_u32 v22, v152, v153, v154\n"
      "v_max_u32 v22, v22, v155\n"
      "v_cmp_eq_u32 vcc, -1, v22\n"
      "s_cbranch_vccnz L_f1_6\n"
      "s_branch L_k1_6\n"
      "L_f1_7:\n"
      "s_movk_i32 s52, 7\n"
      "s_movk_i32 s54, 9\n"
      "s_call_b64 s[12:13], L_rec1\n"
      "v_max3_u32 v22, v156, v157, v158\n"
      "v_max_u32 v22, v22, v159\n"
      "v_cmp_eq_u32 vcc, -1, v22\n"
      "s_cbranch_vccnz L_f1_7\n"
      "s_branch L_k1_7\n"
      "L_f1_8:\n"
      "s_movk_i32 s52, 8\n"
      "s_movk_i32 s54, 9\n"
      "s_call_b64 s[12:13], L_rec1\n"
      "v_max3_u32 v22, v160, v161, v162\n"
      "v_max_u32 v22, v22, v163\n"
      "v_cmp_eq_u32 vcc, -1, v22\n"
      "s_cbranch_vccnz L_f1_8\n"
      "s_branch L_k1_8\n"
      "L_f1_9:\n"
      "s_movk_i32 s52, 9\n"
      "s_movk_i32 s54, 9\n"
      "s_call_b64 s[12:13], L_rec1\n"
      "v_max3_u32 v22, v164, v165, v166\n"
      "v_max_u32 v22, v22, v167\n"
      "v_cmp_eq_u32 vcc, -1, v22\n"
      "s_cbranch_vccnz L_f1_9\n"
      "s_branch L_k1_9\n"
      "L_f1_10:\n"
      "s_movk_i32 s52, 10\n"
      "s_movk_i32 s54, 9\n"
      "s_call_b64 s[12:13], L_rec1\n"
      "v_max3_u32 v22, v168, v169, v170\n"
      "v_max_u32 v22, v22, v171\n"
      "v_cmp_eq_u32 vcc, -1, v22\n"
      "s_cbranch_vccnz L_f1_10\n"
      "s_branch L_k1_10\n"
      "L_f1_11:\n"
      "s_movk_i32 s52, 11\n"
      "s_movk_i32 s54, 9\n"
      "s_call_b64 s[12:13], L_rec1\n"
      "v_max3_u32 v22, v172, v173, v174\n"
      "v_max_u32 v22, v22, v175\n"
      "v_cmp_eq_u32 vcc, -1, v22\n"
      "s_cbranch_vccnz L_f1_11\n"
      "s_branch L_k1_11\n"
      "L_rec1:\n"
      "s_sub_u32 s46, s46, 1\n"
      "s_cbranch_scc1 L_giveup\n"
      "s_sleep 2\n"
      "s_add_u32 s43, s49, s52\n"
      "s_and_b32 s43, s43, 31\n"
      "s_lshl_b32 s43, s43, 10\n"
      "s_mul_i32 s52, s52, 32\n"
      "s_getpc_b64 s[14:15]\n"
      "L_rec1_pc:\n"
      "s_add_u32 s52, s52, L_rec1_t-L_rec1_pc\n"
      "s_add_u32 s14, s14, s52\n"
      "s_addc_u32 s15, s15, 0\n"
      "s_setpc_b64 s[14:15]\n"
      "L_rec1_t:\n"
      "buffer_load_dwordx4 v[128:131], v13, s[64:67], s43 offen sc1\n"
      "s_add_u32 s43, s43, 0x400\n"
      "s_and_b32 s43, s43, 0x7fff\n"
      "s_sub_u32 s54, s54, 1\n"
      "s_cbranch_scc1 L_rec1_x\n"
      "buffer_load_dwordx4 v[132:135], v13, s[64:67], s43 offen sc1\n"
      "s_add_u32 s43, s43, 0x400\n"
      "s_and_b32 s43, s43, 0x7fff\n"
      "s_sub_u32 s54, s54, 1\n"
      "s_cbranch_scc1 L_rec1_x\n"
      "buffer_load_dwordx4 v[136:139], v13, s[64:67], s43 offen sc1\n"
      "s_add_u32 s43, s43, 0x400\n"
      "s_and_b32 s43, s43, 0x7fff\n"
      "s_sub_u32 s54, s54, 1\n"
      "s_cbranch_scc1 L_rec1_x\n"
      "buffer_load_dwordx4 v[140:143], v13, s[64:67], s43 offen sc1\n"
      "s_add_u32 s43, s43, 0x400\n"
      "s_and_b32 s43, s43, 0x7fff\n"
      "s_sub_u32 s54, s54, 1\n"
      "s_cbranch_scc1 L_rec1_x\n"
      "buffer_load_dwordx4 v[144:147], v13, s[64:67], s43 offen sc1\n"
      "s_add_u32 s43, s43, 0x400\n"
      "s_and_b32 s43, s43, 0x7fff\n"
      "s_sub_u32 s54, s54, 1\n"
      "s_cbranch_scc1 L_rec1_x\n"
      "buffer_load_dwordx4 v[148:151], v13, s[64:67], s43 offen sc1\n"
      "s_add_u32 s43, s43, 0x400\n"
      "s_and_b32 s43, s43, 0x7fff\n"
      "s_sub_u32 s54, s54, 1\n"
      "s_cbranch_scc1 L_rec1_x\n"
      "buffer_load_dwordx4 v[152:155], v13, s[64:67], s43 offen sc1\n"
      "s_add_u32 s43, s43, 0x400\n"
      "s_and_b32 s43, s43, 0x7fff\n"
      "s_sub_u32 s54, s54, 1\n"
      "s_cbranch_scc1 L_rec1_x\n"
      "buffer_load_dwordx4 v[156:159], v13, s[64:67], s43 offen sc1\n"
      "s_add_u32 s43, s43, 0x400\n"
      "s_and_b32 s43, s43, 0x7fff\n"
      "s_sub_u32 s54, s54, 1\n"
      "s_cbranch_scc1 L_rec1_x\n"
      "buffer_load_dwordx4 v[160:163], v13, s[64:67], s43 offen sc1\n"
      "s_add_u32 s43, s43, 0x400\n"
      "s_and_b32 s43, s43, 0x7fff\n"
      "s_sub_u32 s54, s54, 1\n"
      "s_cbranch_scc1 L_rec1_x\n"
      "buffer_load_dwordx4 v[164:167], v13, s[64:67], s43 offen sc1\n"
      "s_add_u32 s43, s43, 0x400\n"
      "s_and_b32 s43, s43, 0x7fff\n"
      "s_sub_u32 s54, s54, 1\n"
      "s_cbranch_scc1 L_rec1_x\n"
      "buffer_load_dwordx4 v[168:171], v13, s[64:67], s43 offen sc1\n"
      "s_add_u32 s43, s43, 0x400\n"
      "s_and_b32 s43, s43, 0x7fff\n"
      "s_sub_u32 s54, s54, 1\n"
      "s_cbranch_scc1 L_rec1_x\n"
      "buffer_load_dwordx4 v[172:175], v13, s[64:67], s43 offen sc1\n"
      "s_add_u32 s43, s43, 0x400\n"
      "s_and_b32 s43, s43, 0x7fff\n"
      "s_sub_u32 s54, s54, 1\n"
      "s_cbranch_scc1 L_rec1_x\n"
      "buffer_load_dwordx4 v[176:179], v13, s[64:67], s43 offen sc1\n"
      "s_add_u32 s43, s43, 0x400\n"
      "s_and_b32 s43, s43, 0x7fff\n"
      "s_sub_u32 s54, s54, 1\n"
      "s_cbranch_scc1 L_rec1_x\n"
      "buffer_load_dwordx4 v[180:183], v13, s[64:67], s43 offen sc1\n"
      "s_add_u32 s43, s43, 0x400\n"
      "s_and_b32 s43, s43, 0x7fff\n"
      "s_sub_u32 s54, s54, 1\n"
      "s_cbranch_scc1 L_rec1_x\n"
      "buffer_load_dwordx4 v[184:187], v13, s[64:67], s43 offen sc1\n"
      "s_add_u32 s43, s43, 0x400\n"
      "s_and_b32 s43, s43, 0x7fff\n"
      "s_sub_u32 s54, s54, 1\n"
      "s_cbranch_scc1 L_rec1_x\n"
      "buffer_load_dwordx4 v[188:191], v13, s[64:67], s43 offen sc1\n"
      "s_add_u32 s43, s43, 0x400\n"
      "s_and_b32 s43, s43, 0x7fff\n"
      "s_sub_u32 s54, s54, 1\n"
      "s_cbranch_scc1 L_rec1_x\n"
      "buffer_load_dwordx4 v[192:195], v13, s[64:67], s43 offen sc1\n"
      "s_add_u32 s43, s43, 0x400\n"
      "s_and_b32 s43, s43, 0x7fff\n"
      "s_sub_u32 s54, s54, 1\n"
      "s_cbranch_scc1 L_rec1_x\n"
      "buffer_load_dwordx4 v[196:199], v13, s[64:67], s43 offen sc1\n"
      "s_add_u32 s43, s43, 0x400\n"
      "s_and_b32 s43, s43, 0x7fff\n"
      "s_sub_u32 s54, s54, 1\n"
      "s_cbranch_scc1 L_rec1_x\n"
      "buffer_load_dwordx4 v[200:203], v13, s[64:67], s43 offen sc1\n"
      "s_add_u32 s43, s43, 0x400\n"
      "s_and_b32 s43, s43, 0x7fff\n"
      "s_sub_u32 s54, s54, 1\n"
      "s_cbranch_scc1 L_rec1_x\n"
      "buffer_load_dwordx4 v[204:207], v13, s[64:67], s43 offen sc1\n"
      "s_add_u32 s43, s43, 0x400\n"
      "s_and_b32 s43, s43, 0x7fff\n"
      "s_sub_u32 s54, s54, 1\n"
      "s_cbranch_scc1 L_rec1_x\n"
      "buffer_load_dwordx4 v[208:211], v13, s[64:67], s43 offen sc1\n"
      "s_add_u32 s43, s43, 0x400\n"
      "s_and_b32 s43, s43, 0x7fff\n"
      "s_sub_u32 s54, s54, 1\n"
      "s_cbranch_scc1 L_rec1_x\n"
      "buffer_load_dwordx4 v[212:215], v13, s[64:67], s43 offen sc1\n"
      "s_add_u32 s43, s43, 0x400\n"
      "s_and_b32 s43, s43, 0x7fff\n"
      "s_sub_u32 s54, s54, 1\n"
      "s_cbranch_scc1 L_rec1_x\n"
      "buffer_load_dwordx4 v[216:219], v13, s[64:67], s43 offen sc1\n"
      "s_add_u32 s43, s43, 0x400\n"
      "s_and_b32 s43, s43, 0x7fff\n"
      "s_sub_u32 s54, s54, 1\n"
      "s_cbranch_scc1 L_rec1_x\n"
      "buffer_load_dwordx4 v[220:223], v13, s[64:67], s43 offen sc1\n"
      "s_add_u32 s43, s43, 0x400\n"
      "s_and_b32 s43, s43, 0x7fff\n"
      "s_sub_u32 s54, s54, 1\n"
      "s_cbranch_scc1 L_rec1_x\n"
      "buffer_load_dwordx4 v[224:227], v13, s[64:67], s43 offen sc1\n"
      "s_add_u32 s43, s43, 0x400\n"
      "s_and_b32 s43, s43, 0x7fff\n"
      "s_sub_u32 s54, s54, 1\n"
      "s_cbranch_scc1 L_rec1_x\n"
      "buffer_load_dwordx4 v[228:231], v13, s[64:67], s43 offen sc1\n"
      "s_add_u32 s43, s43, 0x400\n"
      "s_and_b32 s43, s43, 0x7fff\n"
      "s_sub_u32 s54, s54, 1\n"
      "s_cbranch_scc1 L_rec1_x\n"
      "buffer_load_dwordx4 v[232:235], v13, s[64:67], s43 offen sc1\n"
      "s_add_u32 s43, s43, 0x400\n"
      "s_and_b32 s43, s43, 0x7fff\n"
      "s_sub_u32 s54, s54, 1\n"
      "s_cbranch_scc1 L_rec1_x\n"
      "buffer_load_dwordx4 v[236:239], v13, s[64:67], s43 offen sc1\n"
      "s_add_u32 s43, s43, 0x400\n"
      "s_and_b32 s43, s43, 0x7fff\n"
      "s_sub_u32 s54, s54, 1\n"
      "s_cbranch_scc1 L_rec1_x\n"
      "buffer_load_dwordx4 v[240:243], v13, s[64:67], s43 offen sc1\n"
      "s_add_u32 s43, s43, 0x400\n"
      "s_and_b32 s43, s43, 0x7fff\n"
      "s_sub_u32 s54, s54, 1\n"
      "s_cbranch_scc1 L_rec1_x\n"
      "buffer_load_dwordx4 v[244:247], v13, s[64:67], s43 offen sc1\n"
      "s_add_u32 s43, s43, 0x400\n"
      "s_and_b32 s43, s43, 0x7fff\n"
      "s_sub_u32 s54, s54, 1\n"
      "s_cbranch_scc1 L_rec1_x\n"
      "buffer_load_dwordx4 v[248:251], v13, s[64:67], s43 offen sc1\n"
      "s_add_u32 s43, s43, 0x400\n"
      "s_and_b32 s43, s43, 0x7fff\n"
      "s_sub_u32 s54, s54, 1\n"
      "s_cbranch_scc1 L_rec1_x\n"
      "buffer_load_dwordx4 v[252:255], v13, s[64:67], s43 offen sc1\n"
      "s_add_u32 s43, s43, 0x400\n"
      "s_and_b32 s43, s43, 0x7fff\n"
      "s_sub_u32 s54, s54, 1\n"
      "s_cbranch_scc1 L_rec1_x\n"
      "L_rec1_x:\n"
      "s_waitcnt vmcnt(0)\n"
      "s_setpc_b64 s[12:13]\n"
      "L_nan1:\n"
      "s_mov_b32 s53, 0\n"
      "L_nan1_l:\n"
      "v_mov_b32 v22, 0\n"
      "v_max3_u32 v22, v22, v128, v129\n"
      "v_max3_u32 v22, v22, v130, v131\n"
      "v_max3_u32 v22, v22, v132, v133\n"
      "v_max3_u32 v22, v22, v134, v135\n"
      "v_max3_u32 v22, v22, v136, v137\n"
      "v_max3_u32 v22, v22, v138, v139\n"
      "v_max3_u32 v22, v22, v140, v141\n"
      "v_max3_u32 v22, v22, v142, v143\n"
      "v_max3_u32 v22, v22, v144, v145\n"
      "v_max3_u32 v22, v22, v146, v147\n"
      "v_max3_u32 v22, v22, v148, v149\n"
      "v_max3_u32 v22, v22, v150, v151\n"
      "v_max3_u32 v22, v22, v152, v153\n"
      "v_max3_u32 v22, v22, v154, v155\n"
      "v_max3_u32 v22, v22, v156, v157\n"
      "v_max3_u32 v22, v22, v158, v159\n"
      "v_max3_u32 v22, v22, v160, v161\n"
      "v_max3_u32 v22, v22, v162, v163\n"
      "v_max3_u32 v22, v22, v164, v165\n"
      "v_max3_u32 v22, v22, v166, v167\n"
      "v_max3_u32 v22, v22, v168, v169\n"
      "v_max3_u32 v22, v22, v170, v171\n"
      "v_max3_u32 v22, v22, v172, v173\n"
      "v_max3_u32 v22, v22, v174, v175\n"
      "v_max3_u32 v22, v22, v176, v177\n"
      "v_max3_u32 v22, v22, v178, v179\n"
      "v_max3_u32 v22, v22, v180, v181\n"
      "v_max3_u32 v22, v22, v182, v183\n"
      "v_max3_u32 v22, v22, v184, v185\n"
      "v_max3_u32 v22, v22, v186, v187\n"
      "v_max3_u32 v22, v22, v188, v189\n"
      "v_max3_u32 v22, v22, v190, v191\n"
      "v_max3_u32 v22, v22, v192, v193\n"
      "v_max3_u32 v22, v22, v194, v195\n"
      "v_max3_u32 v22, v22, v196, v197\n"
      "v_max3_u32 v22, v22, v198, v199\n"
      "v_max3_u32 v22, v22, v200, v201\n"
      "v_max3_u32 v22, v22, v202, v203\n"
      "v_max3_u32 v22, v22, v204, v205\n"
      "v_max3_u32 v22, v22, v206, v207\n"
      "v_max3_u32 v22, v22, v208, v209\n"
      "v_max3_u32 v22, v22, v210, v211\n"
      "v_max3_u32 v22, v22, v212, v213\n"
      "v_max3_u32 v22, v22, v214, v215\n"
      "v_max3_u32 v22, v22, v216, v217\n"
      "v_max3_u32 v22, v22, v218, v219\n"
      "v_max3_u32 v22, v22, v220, v221\n"
      "v_max3_u32 v22, v22, v222, v223\n"
      "v_max3_u32 v22, v22, v224, v225\n"
      "v_max3_u32 v22, v22, v226, v227\n"
      "v_max3_u32 v22, v22, v228, v229\n"
      "v_max3_u32 v22, v22, v230, v231\n"
      "v_max3_u32 v22, v22, v232, v233\n"
      "v_max3_u32 v22, v22, v234, v235\n"
      "v_max3_u32 v22, v22, v236, v237\n"
      "v_max3_u32 v22, v22, v238, v239\n"
      "v_max3_u32 v22, v22, v240, v241\n"
      "v_max3_u32 v22, v22, v242, v243\n"
      "v_max3_u32 v22, v22, v244, v245\n"
      "v_max3_u32 v22, v22, v246, v247\n"
      "v_max3_u32 v22, v22, v248, v249\n"
      "v_max3_u32 v22, v22, v250, v251\n"
      "v_max3_u32 v22, v22, v252, v253\n"
      "v_max3_u32 v22, v22, v254, v255\n"
      "v_cmp_eq_u32 vcc, -1, v22\n"
      "s_cbranch_vccz L_nan1_ok\n"
      "s_mov_b32 s53, 1\n"
      "s_movk_i32 s52, 0\n"
      "s_movk_i32 s54, 31\n"
      "s_call_b64 s[12:13], L_rec1\n"
      "s_branch L_nan1_l\n"
      "L_nan1_ok:\n"
      "s_cmp_eq_u32 s53, 0\n"
      "s_cbranch_scc1 L_nan1_x\n"
      "v_mfma_f32_32x32x16_bf16 v[80:95], a[4:7], v[128:131], 0\n"
      "v_mfma_f32_32x32x16_bf16 v[64:79], a[0:3], v[128:131], 0\n"
      "v_mfma_f32_32x32x16_bf16 v[80:95], a[12:15], v[132:135], v[80:95]\n"
      "v_mfma_f32_32x32x16_bf16 v[64:79], a[8:11], v[132:135], v[64:79]\n"
      "v_mfma_f32_32x32x16_bf16 v[80:95], a[20:23], v[136:139], v[80:95]\n"
      "v_mfma_f32_32x32x16_bf16 v[64:79], a[16:19], v[136:139], v[64:79]\n"
      "v_mfma_f32_32x32x16_bf16 v[80:95], a[28:31], v[140:143], v[80:95]\n"
      "v_mfma_f32_32x32x16_bf16 v[64:79], a[24:27], v[140:143], v[64:79]\n"
      "v_mfma_f32_32x32x16_bf16 v[80:95], a[36:39], v[144:147], v[80:95]\n"
      "v_mfma_f32_32x32x16_bf16 v[64:79], a[32:35], v[144:147], v[64:79]\n"
      "v_mfma_f32_32x32x16_bf16 v[80:95], a[44:47], v[148:151], v[80:95]\n"
      "v_mfma_f32_32x32x16_bf16 v[64:79], a[40:43], v[148:151], v[64:79]\n"
      "v_mfma_f32_32x32x16_bf16 v[80:95], a[52:55], v[152:155], v[80:95]\n"
      "v_mfma_f32_32x32x16_bf16 v[64:79], a[48:51], v[152:155], v[64:79]\n"
      "v_mfma_f32_32x32x16_bf16 v[80:95], a[60:63], v[156:159], v[80:95]\n"
      "v_mfma_f32_32x32x16_bf16 v[64:79], a[56:59], v[156:159], v[64:79]\n"
      "v_mfma_f32_32x32x16_bf16 v[80:95], a[68:71], v[160:163], v[80:95]\n"
      "v_mfma_f32_32x32x16_bf16 v[64:79], a[64:67], v[160:163], v[64:79]\n"
      "v_mfma_f32_32x32x16_bf16 v[80:95], a[76:79], v[164:167], v[80:95]\n"
      "v_mfma_f32_32x32x16_bf16 v[64:79], a[72:75], v[164:167], v[64:79]\n"
      "v_mfma_f32_32x32x16_bf16 v[80:95], a[84:87], v[168:171], v[80:95]\n"
      "v_mfma_f32_32x32x16_bf16 v[64:79], a[80:83], v[168:171], v[64:79]\n"
      "v_mfma_f32_32x32x16_bf16 v[80:95], a[92:95], v[172:175], v[80:95]\n"
      "v_mfma_f32_32x32x16_bf16 v[64:79], a[88:91], v[172:175], v[64:79]\n"
      "v_mfma_f32_32x32x16_bf16 v[80:95], a[100:103], v[176:179], v[80:95]\n"
      "v_mfma_f32_32x32x16_bf16 v[64:79], a[96:99], v[176:179], v[64:79]\n"
      "v_mfma_f32_32x32x16_bf16 v[80:95], a[108:111], v[180:183], v[80:95]\n"
      "v_mfma_f32_32x32x16_bf16 v[64:79], a[104:107], v[180:183], v[64:79]\n"
      "v_mfma_f32_32x32x16_bf16 v[80:95], a[116:119], v[184:187], v[80:95]\n"
      "v_mfma_f32_32x32x16_bf16 v[64:79], a[112:115], v[184:187], v[64:79]\n"
      "v_mfma_f32_32x32x16_bf16 v[80:95], a[124:127], v[188:191], v[80:95]\n"
      "v_mfma_f32_32x32x16_bf16 v[64:79], a[120:123], v[188:191], v[64:79]\n"
      "v_mfma_f32_32x32x16_bf16 v[80:95], a[132:135], v[192:195], v[80:95]\n"
      "v_mfma_f32_32x32x16_bf16 v[64:79], a[128:131], v[192:195], v[64:79]\n"
      "v_mfma_f32_32x32x16_bf16 v[80:95], a[140:143], v[196:199], v[80:95]\n"
      "v_mfma_f32_32x32x16_bf16 v[64:79], a[136:139], v[196:199], v[64:79]\n"
      "v_mfma_f32_32x32x16_bf16 v[80:95], a[148:151], v[200:203], v[80:95]\n"
      "v_mfma_f32_32x32x16_bf16 v[64:79], a[144:147], v[200:203], v[64:79]\n"
      "v_mfma_f32_32x32x16_bf16 v[80:95], a[156:159], v[204:207], v[80:95]\n"
      "v_mfma_f32_32x32x16_bf16 v[64:79], a[152:155], v[204:207], v[64:79]\n"
      "v_mfma_f32_32x32x16_bf16 v[80:95], a[164:167], v[208:211], v[80:95]\n"
      "v_mfma_f32_32x32x16_bf16 v[64:79], a[160:163], v[208:211], v[64:79]\n"
      "v_mfma_f32_32x32x16_bf16 v[80:95], a[172:175], v[212:215], v[80:95]\n"
      "v_mfma_f32_32x32x16_bf16 v[64:79], a[168:171], v[212:215], v[64:79]\n"
      "v_mfma_f32_32x32x16_bf16 v[80:95], a[180:183], v[216:219], v[80:95]\n"
      "v_mfma_f32_32x32x16_bf16 v[64:79], a[176:179], v[216:219], v[64:79]\n"
      "v_mfma_f32_32x32x16_bf16 v[80:95], a[188:191], v[220:223], v[80:95]\n"
      "v_mfma_f32_32x32x16_bf16 v[64:79], a[184:187], v[220:223], v[64:79]\n"
      "v_mfma_f32_32x32x16_bf16 v[80:95], a[196:199], v[224:227], v[80:95]\n"
      "v_mfma_f32_32x32x16_bf16 v[64:79], a[192:195], v[224:227], v[64:79]\n"
      "v_mfma_f32_32x32x16_bf16 v[80:95], a[204:207], v[228:231], v[80:95]\n"
      "v_mfma_f32_32x32x16_bf16 v[64:79], a[200:203], v[228:231], v[64:79]\n"
      "v_mfma_f32_32x32x16_bf16 v[80:95], a[212:215], v[232:235], v[80:95]\n"
      "v_mfma_f32_32x32x16_bf16 v[64:79], a[208:211], v[232:235], v[64:79]\n"
      "v_mfma_f32_32x32x16_bf16 v[80:95], a[220:223], v[236:239], v[80:95]\n"
      "v_mfma_f32_32x32x16_bf16 v[64:79], a[216:219], v[236:239], v[64:79]\n"
      "v_mfma_f32_32x32x16_bf16 v[80:95], a[228:231], v[240:243], v[80:95]\n"
      "v_mfma_f32_32x32x16_bf16 v[64:79], a[224:227], v[240:243], v[64:79]\n"
      "v_mfma_f32_32x32x16_bf16 v[80:95], a[236:239], v[244:247], v[80:95]\n"
      "v_mfma_f32_32x32x16_bf16 v[64:79], a[232:235], v[244:247], v[64:79]\n"
      "v_mfma_f32_32x32x16_bf16 v[80:95], a[244:247], v[248:251], v[80:95]\n"
      "v_mfma_f32_32x32x16_bf16 v[64:79], a[240:243], v[248:251], v[64:79]\n"
      "v_mfma_f32_32x32x16_bf16 v[80:95], a[252:255], v[252:255], v[80:95]\n"
      "v_mfma_f32_32x32x16_bf16 v[64:79], a[248:251], v[252:255], v[64:79]\n"
      "s_nop 12\n"
      "s_branch L_nan1_x\n"
      "L_f2_0:\n"
      "s_movk_i32 s52, 0\n"
      "s_movk_i32 s54, 9\n"
      "s_call_b64 s[12:13], L_rec2\n"
      "v_max3_u32 v22, v128, v129, v130\n"
      "v_max_u32 v22, v22, v131\n"
      "v_cmp_eq_u32 vcc, -1, v22\n"
      "s_cbranch_vccnz L_f2_0\n"
      "s_branch L_k2_0\n"
      "L_f2_1:\n"
      "s_movk_i32 s52, 1\n"
      "s_movk_i32 s54, 9\n"
      "s_call_b64 s[12:13], L_rec2\n"
      "v_max3_u32 v22, v132, v133, v134\n"
      "v_max_u32 v22, v22, v135\n"
      "v_cmp_eq_u32 vcc, -1, v22\n"
      "s_cbranch_vccnz L_f2_1\n"
      "s_branch L_k2_1\n"
      "L_f2_2:\n"
      "s_movk_i32 s52, 2\n"
      "s_movk_i32 s54, 9\n"
      "s_call_b64 s[12:13], L_rec2\n"
      "v_max3_u32 v22, v136, v137, v138\n"
      "v_max_u32 v22, v22, v139\n"
      "v_cmp_eq_u32 vcc, -1, v22\n"
      "s_cbranch_vccnz L_f2_2\n"
      "s_branch L_k2_2\n"
      "L_f2_3:\n"
      "s_movk_i32 s52, 3\n"
      "s_movk_i32 s54, 9\n"
      "s_call_b64 s[12:13], L_rec2\n"
      "v_max3_u32 v22, v140, v141, v142\n"
      "v_max_u32 v22, v22, v143\n"
      "v_cmp_eq_u32 vcc, -1, v22\n"
      "s_cbranch_vccnz L_f2_3\n"
      "s_branch L_k2_3\n"
      "L_f2_4:\n"
      "s_movk_i32 s52, 4\n"
      "s_movk_i32 s54, 9\n"
      "s_call_b64 s[12:13], L_rec2\n"
      "v_max3_u32 v22, v144, v145, v146\n"
      "v_max_u32 v22, v22, v147\n"
      "v_cmp_eq_u32 vcc, -1, v22\n"
      "s_cbranch_vccnz L_f2_4\n"
      "s_branch L_k2_4\n"
      "L_f2_5:\n"
      "s_movk_i32 s52, 5\n"
      "s_movk_i32 s54, 9\n"
      "s_call_b64 s[12:13], L_rec2\n"
      "v_max3_u32 v22, v148, v149, v150\n"
      "v_max_u32 v22, v22, v151\n"
      "v_cmp_eq_u32 vcc, -1, v22\n"
      "s_cbranch_vccnz L_f2_5\n"
      "s_branch L_k2_5\n"
      "L_f2_6:\n"
      "s_movk_i32 s52, 6\n"
      "s_movk_i32 s54, 9\n"
      "s_call_b64 s[12:13], L_rec2\n"
      "v_max3_u32 v22, v152, v153, v154\n"
      "v_max_u32 v22, v22, v155\n"
      "v_cmp_eq_u32 vcc, -1, v22\n"
      "s_cbranch_vccnz L_f2_6\n"
      "s_branch L_k2_6\n"
      "L_f2_7:\n"
      "s_movk_i32 s52, 7\n"
      "s_movk_i32 s54, 9\n"
      "s_call_b64 s[12:13], L_rec2\n"
      "v_max3_u32 v22, v156, v157, v158\n"
      "v_max_u32 v22, v22, v159\n"
      "v_cmp_eq_u32 vcc, -1, v22\n"
      "s_cbranch_vccnz L_f2_7\n"
      "s_branch L_k2_7\n"
      "L_f2_8:\n"
      "s_movk_i32 s52, 8\n"
      "s_movk_i32 s54, 9\n"
      "s_call_b64 s[12:13], L_rec2\n"
      "v_max3_u32 v22, v160, v161, v162\n"
      "v_max_u32 v22, v22, v163\n"
      "v_cmp_eq_u32 vcc, -1, v22\n"
      "s_cbranch_vccnz L_f2_8\n"
      "s_branch L_k2_8\n"
      "L_f2_9:\n"
      "s_movk_i32 s52, 9\n"
      "s_movk_i32 s54, 9\n"
      "s_call_b64 s[12:13], L_rec2\n"
      "v_max3_u32 v22, v164, v165, v166\n"
      "v_max_u32 v22, v22, v167\n"
      "v_cmp_eq_u32 vcc, -1, v22\n"
      "s_cbranch_vccnz L_f2_9\n"
      "s_branch L_k2_9\n"
      "L_f2_10:\n"
      "s_movk_i32 s52, 10\n"
      "s_movk_i32 s54, 9\n"
      "s_call_b64 s[12:13], L_rec2\n"
      "v_max3_u32 v22, v168, v169, v170\n"
      "v_max_u32 v22, v22, v171\n"
      "v_cmp_eq_u32 vcc, -1, v22\n"
      "s_cbranch_vccnz L_f2_10\n"
      "s_branch L_k2_10\n"
      "L_f2_11:\n"
      "s_movk_i32 s52, 11\n"
      "s_movk_i32 s54, 9\n"
      "s_call_b64 s[12:13], L_rec2\n"
      "v_max3_u32 v22, v172, v173, v174\n"
      "v_max_u32 v22, v22, v175\n"
      "v_cmp_eq_u32 vcc, -1, v22\n"
      "s_cbranch_vccnz L_f2_11\n"
      "s_branch L_k2_11\n"
      "L_rec2:\n"
      "s_sub_u32 s46, s46, 1\n"
      "s_cbranch_scc1 L_giveup\n"
      "s_sleep 2\n"
      "s_add_u32 s43, s50, s52\n"
      "s_and_b32 s43, s43, 31\n"
      "s_lshl_b32 s43, s43, 10\n"
      "s_mul_i32 s52, s52, 32\n"
      "s_getpc_b64 s[14:15]\n"
      "L_rec2_pc:\n"
      "s_add_u32 s52, s52, L_rec2_t-L_rec2_pc\n"
      "s_add_u32 s14, s14, s52\n"
      "s_addc_u32 s15, s15, 0\n"
      "s_setpc_b64 s[14:15]\n"
      "L_rec2_t:\n"
      "buffer_load_dwordx4 v[128:131], v13, s[64:67], s43 offen sc1\n"
      "s_add_u32 s43, s43, 0x400\n"
      "s_and_b32 s43, s43, 0x7fff\n"
      "s_sub_u32 s54, s54, 1\n"
      "s_cbranch_scc1 L_rec2_x\n"
      "buffer_load_dwordx4 v[132:135], v13, s[64:67], s43 offen sc1\n"
      "s_add_u32 s43, s43, 0x400\n"
      "s_and_b32 s43, s43, 0x7fff\n"
      "s_sub_u32 s54, s54, 1\n"
      "s_cbranch_scc1 L_rec2_x\n"
      "buffer_load_dwordx4 v[136:139], v13, s[64:67], s43 offen sc1\n"
      "s_add_u32 s43, s43, 0x400\n"
      "s_and_b32 s43, s43, 0x7fff\n"
      "s_sub_u32 s54, s54, 1\n"
      "s_cbranch_scc1 L_rec2_x\n"
      "buffer_load_dwordx4 v[140:143], v13, s[64:67], s43 offen sc1\n"
      "s_add_u32 s43, s43, 0x400\n"
      "s_and_b32 s43, s43, 0x7fff\n"
      "s_sub_u32 s54, s54, 1\n"
      "s_cbranch_scc1 L_rec2_x\n"
      "buffer_load_dwordx4 v[144:147], v13, s[64:67], s43 offen sc1\n"
      "s_add_u32 s43, s43, 0x400\n"
      "s_and_b32 s43, s43, 0x7fff\n"
      "s_sub_u32 s54, s54, 1\n"
      "s_cbranch_scc1 L_rec2_x\n"
      "buffer_load_dwordx4 v[148:151], v13, s[64:67], s43 offen sc1\n"
      "s_add_u32 s43, s43, 0x400\n"
      "s_and_b32 s43, s43, 0x7fff\n"
      "s_sub_u32 s54, s54, 1\n"
      "s_cbranch_scc1 L_rec2_x\n"
      "buffer_load_dwordx4 v[152:155], v13, s[64:67], s43 offen sc1\n"
      "s_add_u32 s43, s43, 0x400\n"
      "s_and_b32 s43, s43, 0x7fff\n"
      "s_sub_u32 s54, s54, 1\n"
      "s_cbranch_scc1 L_rec2_x\n"
      "buffer_load_dwordx4 v[156:159], v13, s[64:67], s43 offen sc1\n"
      "s_add_u32 s43, s43, 0x400\n"
      "s_and_b32 s43, s43, 0x7fff\n"
      "s_sub_u32 s54, s54, 1\n"
      "s_cbranch_scc1 L_rec2_x\n"
      "buffer_load_dwordx4 v[160:163], v13, s[64:67], s43 offen sc1\n"
      "s_add_u32 s43, s43, 0x400\n"
      "s_and_b32 s43, s43, 0x7fff\n"
      "s_sub_u32 s54, s54, 1\n"
      "s_cbranch_scc1 L_rec2_x\n"
      "buffer_load_dwordx4 v[164:167], v13, s[64:67], s43 offen sc1\n"
      "s_add_u32 s43, s43, 0x400\n"
      "s_and_b32 s43, s43, 0x7fff\n"
      "s_sub_u32 s54, s54, 1\n"
      "s_cbranch_scc1 L_rec2_x\n"
      "buffer_load_dwordx4 v[168:171], v13, s[64:67], s43 offen sc1\n"
      "s_add_u32 s43, s43, 0x400\n"
      "s_and_b32 s43, s43, 0x7fff\n"
      "s_sub_u32 s54, s54, 1\n"
      "s_cbranch_scc1 L_rec2_x\n"
      "buffer_load_dwordx4 v[172:175], v13, s[64:67], s43 offen sc1\n"
      "s_add_u32 s43, s43, 0x400\n"
      "s_and_b32 s43, s43, 0x7fff\n"
      "s_sub_u32 s54, s54, 1\n"
      "s_cbranch_scc1 L_rec2_x\n"
      "buffer_load_dwordx4 v[176:179], v13, s[64:67], s43 offen sc1\n"
      "s_add_u32 s43, s43, 0x400\n"
      "s_and_b32 s43, s43, 0x7fff\n"
      "s_sub_u32 s54, s54, 1\n"
      "s_cbranch_scc1 L_rec2_x\n"
      "buffer_load_dwordx4 v[180:183], v13, s[64:67], s43 offen sc1\n"
      "s_add_u32 s43, s43, 0x400\n"
      "s_and_b32 s43, s43, 0x7fff\n"
      "s_sub_u32 s54, s54, 1\n"
      "s_cbranch_scc1 L_rec2_x\n"
      "buffer_load_dwordx4 v[184:187], v13, s[64:67], s43 offen sc1\n"
      "s_add_u32 s43, s43, 0x400\n"
      "s_and_b32 s43, s43, 0x7fff\n"
      "s_sub_u32 s54, s54, 1\n"
      "s_cbranch_scc1 L_rec2_x\n"
      "buffer_load_dwordx4 v[188:191], v13, s[64:67], s43 offen sc1\n"
      "s_add_u32 s43, s43, 0x400\n"
      "s_and_b32 s43, s43, 0x7fff\n"
      "s_sub_u32 s54, s54, 1\n"
      "s_cbranch_scc1 L_rec2_x\n"
      "buffer_load_dwordx4 v[192:195], v13, s[64:67], s43 offen sc1\n"
      "s_add_u32 s43, s43, 0x400\n"
      "s_and_b32 s43, s43, 0x7fff\n"
      "s_sub_u32 s54, s54, 1\n"
      "s_cbranch_scc1 L_rec2_x\n"
      "buffer_load_dwordx4 v[196:199], v13, s[64:67], s43 offen sc1\n"
      "s_add_u32 s43, s43, 0x400\n"
      "s_and_b32 s43, s43, 0x7fff\n"
      "s_sub_u32 s54, s54, 1\n"
      "s_cbranch_scc1 L_rec2_x\n"
      "buffer_load_dwordx4 v[200:203], v13, s[64:67], s43 offen sc1\n"
      "s_add_u32 s43, s43, 0x400\n"
      "s_and_b32 s43, s43, 0x7fff\n"
      "s_sub_u32 s54, s54, 1\n"
      "s_cbranch_scc1 L_rec2_x\n"
      "buffer_load_dwordx4 v[204:207], v13, s[64:67], s43 offen sc1\n"
      "s_add_u32 s43, s43, 0x400\n"
      "s_and_b32 s43, s43, 0x7fff\n"
      "s_sub_u32 s54, s54, 1\n"
      "s_cbranch_scc1 L_rec2_x\n"
      "buffer_load_dwordx4 v[208:211], v13, s[64:67], s43 offen sc1\n"
      "s_add_u32 s43, s43, 0x400\n"
      "s_and_b32 s43, s43, 0x7fff\n"
      "s_sub_u32 s54, s54, 1\n"
      "s_cbranch_scc1 L_rec2_x\n"
      "buffer_load_dwordx4 v[212:215], v13, s[64:67], s43 offen sc1\n"
      "s_add_u32 s43, s43, 0x400\n"
      "s_and_b32 s43, s43, 0x7fff\n"
      "s_sub_u32 s54, s54, 1\n"
      "s_cbranch_scc1 L_rec2_x\n"
      "buffer_load_dwordx4 v[216:219], v13, s[64:67], s43 offen sc1\n"
      "s_add_u32 s43, s43, 0x400\n"
      "s_and_b32 s43, s43, 0x7fff\n"
      "s_sub_u32 s54, s54, 1\n"
      "s_cbranch_scc1 L_rec2_x\n"
      "buffer_load_dwordx4 v[220:223], v13, s[64:67], s43 offen sc1\n"
      "s_add_u32 s43, s43, 0x400\n"
      "s_and_b32 s43, s43, 0x7fff\n"
      "s_sub_u32 s54, s54, 1\n"
      "s_cbranch_scc1 L_rec2_x\n"
      "buffer_load_dwordx4 v[224:227], v13, s[64:67], s43 offen sc1\n"
      "s_add_u32 s43, s43, 0x400\n"
      "s_and_b32 s43, s43, 0x7fff\n"
      "s_sub_u32 s54, s54, 1\n"
      "s_cbranch_scc1 L_rec2_x\n"
      "buffer_load_dwordx4 v[228:231], v13, s[64:67], s43 offen sc1\n"
      "s_add_u32 s43, s43, 0x400\n"
      "s_and_b32 s43, s43, 0x7fff\n"
      "s_sub_u32 s54, s54, 1\n"
      "s_cbranch_scc1 L_rec2_x\n"
      "buffer_load_dwordx4 v[232:235], v13, s[64:67], s43 offen sc1\n"
      "s_add_u32 s43, s43, 0x400\n"
      "s_and_b32 s43, s43, 0x7fff\n"
      "s_sub_u32 s54, s54, 1\n"
      "s_cbranch_scc1 L_rec2_x\n"
      "buffer_load_dwordx4 v[236:239], v13, s[64:67], s43 offen sc1\n"
      "s_add_u32 s43, s43, 0x400\n"
      "s_and_b32 s43, s43, 0x7fff\n"
      "s_sub_u32 s54, s54, 1\n"
      "s_cbranch_scc1 L_rec2_x\n"
      "buffer_load_dwordx4 v[240:243], v13, s[64:67], s43 offen sc1\n"
      "s_add_u32 s43, s43, 0x400\n"
      "s_and_b32 s43, s43, 0x7fff\n"
      "s_sub_u32 s54, s54, 1\n"
      "s_cbranch_scc1 L_rec2_x\n"
      "buffer_load_dwordx4 v[244:247], v13, s[64:67], s43 offen sc1\n"
      "s_add_u32 s43, s43, 0x400\n"
      "s_and_b32 s43, s43, 0x7fff\n"
      "s_sub_u32 s54, s54, 1\n"
      "s_cbranch_scc1 L_rec2_x\n"
      "buffer_load_dwordx4 v[248:251], v13, s[64:67], s43 offen sc1\n"
      "s_add_u32 s43, s43, 0x400\n"
      "s_and_b32 s43, s43, 0x7fff\n"
      "s_sub_u32 s54, s54, 1\n"
      "s_cbranch_scc1 L_rec2_x\n"
      "buffer_load_dwordx4 v[252:255], v13, s[64:67], s43 offen sc1\n"
      "s_add_u32 s43, s43, 0x400\n"
      "s_and_b32 s43, s43, 0x7fff\n"
      "s_sub_u32 s54, s54, 1\n"
      "s_cbranch_scc1 L_rec2_x\n"
      "L_rec2_x:\n"
      "s_waitcnt vmcnt(0)\n"
      "s_setpc_b64 s[12:13]\n"
      "L_nan2:\n"
      "s_mov_b32 s53, 0\n"
      "L_nan2_l:\n"
      "v_mov_b32 v22, 0\n"
      "v_max3_u32 v22, v22, v128, v129\n"
      "v_max3_u32 v22, v22, v130, v131\n"
      "v_max3_u32 v22, v22, v132, v133\n"
      "v_max3_u32 v22, v22, v134, v135\n"
      "v_max3_u32 v22, v22, v136, v137\n"
      "v_max3_u32 v22, v22, v138, v139\n"
      "v_max3_u32 v22, v22, v140, v141\n"
      "v_max3_u32 v22, v22, v142, v143\n"
      "v_max3_u32 v22, v22, v144, v145\n"
      "v_max3_u32 v22, v22, v146, v147\n"
      "v_max3_u32 v22, v22, v148, v149\n"
      "v_max3_u32 v22, v22, v150, v151\n"
      "v_max3_u32 v22, v22, v152, v153\n"
      "v_max3_u32 v22, v22, v154, v155\n"
      "v_max3_u32 v22, v22, v156, v157\n"
      "v_max3_u32 v22, v22, v158, v159\n"
      "v_max3_u32 v22, v22, v160, v161\n"
      "v_max3_u32 v22, v22, v162, v163\n"
      "v_max3_u32 v22, v22, v164, v165\n"
      "v_max3_u32 v22, v22, v166, v167\n"
      "v_max3_u32 v22, v22, v168, v169\n"
      "v_max3_u32 v22, v22, v170, v171\n"
      "v_max3_u32 v22, v22, v172, v173\n"
      "v_max3_u32 v22, v22, v174, v175\n"
      "v_max3_u32 v22, v22, v176, v177\n"
      "v_max3_u32 v22, v22, v178, v179\n"
      "v_max3_u32 v22, v22, v180, v181\n"
      "v_max3_u32 v22, v22, v182, v183\n"
      "v_max3_u32 v22, v22, v184, v185\n"
      "v_max3_u32 v22, v22, v186, v187\n"
      "v_max3_u32 v22, v22, v188, v189\n"
      "v_max3_u32 v22, v22, v190, v191\n"
      "v_max3_u32 v22, v22, v192, v193\n"
      "v_max3_u32 v22, v22, v194, v195\n"
      "v_max3_u32 v22, v22, v196, v197\n"
      "v_max3_u32 v22, v22, v198, v199\n"
      "v_max3_u32 v22, v22, v200, v201\n"
      "v_max3_u32 v22, v22, v202, v203\n"
      "v_max3_u32 v22, v22, v204, v205\n"
      "v_max3_u32 v22, v22, v206, v207\n"
      "v_max3_u32 v22, v22, v208, v209\n"
      "v_max3_u32 v22, v22, v210, v211\n"
      "v_max3_u32 v22, v22, v212, v213\n"
      "v_max3_u32 v22, v22, v214, v215\n"
      "v_max3_u32 v22, v22, v216, v217\n"
      "v_max3_u32 v22, v22, v218, v219\n"
      "v_max3_u32 v22, v22, v220, v221\n"
      "v_max3_u32 v22, v22, v222, v223\n"
      "v_max3_u32 v22, v22, v224, v225\n"
      "v_max3_u32 v22, v22, v226, v227\n"
      "v_max3_u32 v22, v22, v228, v229\n"
      "v_max3_u32 v22, v22, v230, v231\n"
      "v_max3_u32 v22, v22, v232, v233\n"
      "v_max3_u32 v22, v22, v234, v235\n"
      "v_max3_u32 v22, v22, v236, v237\n"
      "v_max3_u32 v22, v22, v238, v239\n"
      "v_max3_u32 v22, v22, v240, v241\n"
      "v_max3_u32 v22, v22, v242, v243\n"
      "v_max3_u32 v22, v22, v244, v245\n"
      "v_max3_u32 v22, v22, v246, v247\n"
      "v_max3_u32 v22, v22, v248, v249\n"
      "v_max3_u32 v22, v22, v250, v251\n"
      "v_max3_u32 v22, v22, v252, v253\n"
      "v_max3_u32 v22, v22, v254, v255\n"
      "v_cmp_eq_u32 vcc, -1, v22\n"
      "s_cbranch_vccz L_nan2_ok\n"
      "s_mov_b32 s53, 1\n"
      "s_movk_i32 s52, 0\n"
      "s_movk_i32 s54, 31\n"
      "s_call_b64 s[12:13], L_rec2\n"
      "s_branch L_nan2_l\n"
      "L_nan2_ok:\n"
      "s_cmp_eq_u32 s53, 0\n"
      "s_cbranch_scc1 L_nan2_x\n"
      "v_mfma_f32_32x32x16_bf16 v[80:95], a[4:7], v[128:131], 0\n"
      "v_mfma_f32_32x32x16_bf16 v[64:79], a[0:3], v[128:131], 0\n"
      "v_mfma_f32_32x32x16_bf16 v[80:95], a[12:15], v[132:135], v[80:95]\n"
      "v_mfma_f32_32x32x16_bf16 v[64:79], a[8:11], v[132:135], v[64:79]\n"
      "v_mfma_f32_32x32x16_bf16 v[80:95], a[20:23], v[136:139], v[80:95]\n"
      "v_mfma_f32_32x32x16_bf16 v[64:79], a[16:19], v[136:139], v[64:79]\n"
      "v_mfma_f32_32x32x16_bf16 v[80:95], a[28:31], v[140:143], v[80:95]\n"
      "v_mfma_f32_32x32x16_bf16 v[64:79], a[24:27], v[140:143], v[64:79]\n"
      "v_mfma_f32_32x32x16_bf16 v[80:95], a[36:39], v[144:147], v[80:95]\n"
      "v_mfma_f32_32x32x16_bf16 v[64:79], a[32:35], v[144:147], v[64:79]\n"
      "v_mfma_f32_32x32x16_bf16 v[80:95], a[44:47], v[148:151], v[80:95]\n"
      "v_mfma_f32_32x32x16_bf16 v[64:79], a[40:43], v[148:151], v[64:79]\n"
      "v_mfma_f32_32x32x16_bf16 v[80:95], a[52:55], v[152:155], v[80:95]\n"
      "v_mfma_f32_32x32x16_bf16 v[64:79], a[48:51], v[152:155], v[64:79]\n"
      "v_mfma_f32_32x32x16_bf16 v[80:95], a[60:63], v[156:159], v[80:95]\n"
      "v_mfma_f32_32x32x16_bf16 v[64:79], a[56:59], v[156:159], v[64:79]\n"
      "v_mfma_f32_32x32x16_bf16 v[80:95], a[68:71], v[160:163], v[80:95]\n"
      "v_mfma_f32_32x32x16_bf16 v[64:79], a[64:67], v[160:163], v[64:79]\n"
      "v_mfma_f32_32x32x16_bf16 v[80:95], a[76:79], v[164:167], v[80:95]\n"
      "v_mfma_f32_32x32x16_bf16 v[64:79], a[72:75], v[164:167], v[64:79]\n"
      "v_mfma_f32_32x32x16_bf16 v[80:95], a[84:87], v[168:171], v[80:95]\n"
      "v_mfma_f32_32x32x16_bf16 v[64:79], a[80:83], v[168:171], v[64:79]\n"
      "v_mfma_f32_32x32x16_bf16 v[80:95], a[92:95], v[172:175], v[80:95]\n"
      "v_mfma_f32_32x32x16_bf16 v[64:79], a[88:91], v[172:175], v[64:79]\n"
      "v_mfma_f32_32x32x16_bf16 v[80:95], a[100:103], v[176:179], v[80:95]\n"
      "v_mfma_f32_32x32x16_bf16 v[64:79], a[96:99], v[176:179], v[64:79]\n"
      "v_mfma_f32_32x32x16_bf16 v[80:95], a[108:111], v[180:183], v[80:95]\n"
      "v_mfma_f32_32x32x16_bf16 v[64:79], a[104:107], v[180:183], v[64:79]\n"
      "v_mfma_f32_32x32x16_bf16 v[80:95], a[116:119], v[184:187], v[80:95]\n"
      "v_mfma_f32_32x32x16_bf16 v[64:79], a[112:115], v[184:187], v[64:79]\n"
      "v_mfma_f32_32x32x16_bf16 v[80:95], a[124:127], v[188:191], v[80:95]\n"
      "v_mfma_f32_32x32x16_bf16 v[64:79], a[120:123], v[188:191], v[64:79]\n"
      "v_mfma_f32_32x32x16_bf16 v[80:95], a[132:135], v[192:195], v[80:95]\n"
      "v_mfma_f32_32x32x16_bf16 v[64:79], a[128:131], v[192:195], v[64:79]\n"
      "v_mfma_f32_32x32x16_bf16 v[80:95], a[140:143], v[196:199], v[80:95]\n"
      "v_mfma_f32_32x32x16_bf16 v[64:79], a[136:139], v[196:199], v[64:79]\n"
      "v_mfma_f32_32x32x16_bf16 v[80:95], a[148:151], v[200:203], v[80:95]\n"
      "v_mfma_f32_32x32x16_bf16 v[64:79], a[144:147], v[200:203], v[64:79]\n"
      "v_mfma_f32_32x32x16_bf16 v[80:95], a[156:159], v[204:207], v[80:95]\n"
      "v_mfma_f32_32x32x16_bf16 v[64:79], a[152:155], v[204:207], v[64:79]\n"
      "v_mfma_f32_32x32x16_bf16 v[80:95], a[164:167], v[208:211], v[80:95]\n"
      "v_mfma_f32_32x32x16_bf16 v[64:79], a[160:163], v[208:211], v[64:79]\n"
      "v_mfma_f32_32x32x16_bf16 v[80:95], a[172:175], v[212:215], v[80:95]\n"
      "v_mfma_f32_32x32x16_bf16 v[64:79], a[168:171], v[212:215], v[64:79]\n"
      "v_mfma_f32_32x32x16_bf16 v[80:95], a[180:183], v[216:219], v[80:95]\n"
      "v_mfma_f32_32x32x16_bf16 v[64:79], a[176:179], v[216:219], v[64:79]\n"
      "v_mfma_f32_32x32x16_bf16 v[80:95], a[188:191], v[220:223], v[80:95]\n"
      "v_mfma_f32_32x32x16_bf16 v[64:79], a[184:187], v[220:223], v[64:79]\n"
      "v_mfma_f32_32x32x16_bf16 v[80:95], a[196:199], v[224:227], v[80:95]\n"
      "v_mfma_f32_32x32x16_bf16 v[64:79], a[192:195], v[224:227], v[64:79]\n"
      "v_mfma_f32_32x32x16_bf16 v[80:95], a[204:207], v[228:231], v[80:95]\n"
      "v_mfma_f32_32x32x16_bf16 v[64:79], a[200:203], v[228:231], v[64:79]\n"
      "v_mfma_f32_32x32x16_bf16 v[80:95], a[212:215], v[232:235], v[80:95]\n"
      "v_mfma_f32_32x32x16_bf16 v[64:79], a[208:211], v[232:235], v[64:79]\n"
      "v_mfma_f32_32x32x16_bf16 v[80:95], a[220:223], v[236:239], v[80:95]\n"
      "v_mfma_f32_32x32x16_bf16 v[64:79], a[216:219], v[236:239], v[64:79]\n"
      "v_mfma_f32_32x32x16_bf16 v[80:95], a[228:231], v[240:243], v[80:95]\n"
      "v_mfma_f32_32x32x16_bf16 v[64:79], a[224:227], v[240:243], v[64:79]\n"
      "v_mfma_f32_32x32x16_bf16 v[80:95], a[236:239], v[244:247], v[80:95]\n"
      "v_mfma_f32_32x32x16_bf16 v[64:79], a[232:235], v[244:247], v[64:79]\n"
      "v_mfma_f32_32x32x16_bf16 v[80:95], a[244:247], v[248:251], v[80:95]\n"
      "v_mfma_f32_32x32x16_bf16 v[64:79], a[240:243], v[248:251], v[64:79]\n"
      "v_mfma_f32_32x32x16_bf16 v[80:95], a[252:255], v[252:255], v[80:95]\n"
      "v_mfma_f32_32x32x16_bf16 v[64:79], a[248:251], v[252:255], v[64:79]\n"
      "s_nop 12\n"
      "s_branch L_nan2_x\n"
      "L_giveup:\n"
      "s_endpgm\n"
      "L_hs_slow:\n"
      "s_mov_b64 exec, 0xffff\n"
      "L_hs_l:\n"
      "s_sleep 4\n"
      "buffer_load_dword v29, v24, s[64:67], 0 offen sc1\n"
      "s_waitcnt vmcnt(0)\n"
      "v_cmp_ne_u32 vcc, v28, v29\n"
      "s_cbranch_vccz L_hs_same\n"
      "v_mov_b32 v28, v29\n"
      "v_and_b32 v29, 0xffffff00, v28\n"
      "v_or_b32 v29, 1, v29\n"
      "buffer_store_dword v29, v26, s[64:67], 0 offen sc1\n"
      "L_hs_same:\n"
      "s_nop 1\n"
      "buffer_load_dword v29, v25, s[64:67], 0 offen sc1\n"
      "s_waitcnt vmcnt(0)\n"
      "v_cmp_ne_u32 vcc, s53, v29\n"
      "s_cbranch_vccz L_hs_x\n"
      "s_sub_u32 s46, s46, 1\n"
      "s_cbranch_scc0 L_hs_l\n"
      "s_endpgm\n"
      "L_hs_x:\n"
      "s_mov_b64 exec, -1\n"
      "s_branch L_hs_done\n"
      "L_hs_resend:\n"
      "s_mov_b64 exec, 0xffff\n"
      "v_mov_b32 v28, v29\n"
      "v_and_b32 v29, 0xffffff00, v28\n"
      "v_or_b32 v29, 1, v29\n"
      "buffer_store_dword v29, v26, s[64:67], 0 offen sc1\n"
      "s_mov_b64 exec, -1\n"
      "s_branch L_hs_resent\n"
      :
      : "s"(__builtin_amdgcn_kernarg_segment_ptr()), "s"(__builtin_amdgcn_workgroup_id_x()), "v"(__builtin_amdgcn_workitem_id_x())
      : "memory", "vcc", "scc", "s4", "s5", "s6", "s7", "s8", "s9", "s10", "s11", "s12", "s13", "s14", "s15", "s16",
        "s17", "s18", "s19", "s20", "s21", "s22", "s23", "s24", "s25", "s26", "s27", "s28", "s29", "s30", "s31", "s33",
        "s34", "s35", "s36", "s37", "s38", "s39", "s40", "s41", "s42", "s43", "s44", "s45", "s46", "s47", "s48", "s49",
        "s50", "s51", "s52", "s53", "s54", "s55", "s56", "s57", "s58", "s59", "s60", "s61", "s62", "s63", "s64", "s65",
        "s66", "s67", "s68", "s69", "s70", "s71", "s72", "s73", "s74", "s75", "s76", "s77", "s78", "s79", "s80", "s81",
        "s82", "s83", "s84", "s85", "s86", "s87", "s88", "s89", "s90", "s91", "s92", "s93", "s94", "s95", "s96", "s97",
        "s98", "s99", "v1", "v2", "v3", "v4", "v5", "v6", "v7", "v8", "v9", "v10", "v11", "v12", "v13", "v14",
        "v15", "v16", "v17", "v18", "v19", "v20", "v21", "v22", "v23", "v24", "v25", "v26", "v27", "v28", "v29", "v30",
        "v31", "v32", "v33", "v34", "v35", "v36", "v37", "v38", "v39", "v40", "v41", "v42", "v43", "v44", "v45", "v46",
        "v47", "v48", "v49", "v50", "v51", "v52", "v53", "v54", "v55", "v56", "v57", "v58", "v59", "v60", "v61", "v62",
        "v63", "v64", "v65", "v66", "v67", "v68", "v69", "v70", "v71", "v72", "v73", "v74", "v75", "v76", "v77", "v78",
        "v79", "v80", "v81", "v82", "v83", "v84", "v85", "v86", "v87", "v88", "v89", "v90", "v91", "v92", "v93", "v94",
        "v95", "v96", "v97", "v98", "v99", "v100", "v101", "v102", "v103", "v104", "v105", "v106", "v107", "v108", "v109", "v110",
        "v111", "v112", "v113", "v114", "v115", "v116", "v117", "v118", "v119", "v120", "v121", "v122", "v123", "v124", "v125", "v126",
        "v127", "v128", "v129", "v130", "v131", "v132", "v133", "v134", "v135", "v136", "v137", "v138", "v139", "v140", "v141", "v142",
        "v143", "v144", "v145", "v146", "v147", "v148", "v149", "v150", "v151", "v152", "v153", "v154", "v155", "v156", "v157", "v158",
        "v159", "v160", "v161", "v162", "v163", "v164", "v165", "v166", "v167", "v168", "v169", "v170", "v171", "v172", "v173", "v174",
        "v175", "v176", "v177", "v178", "v179", "v180", "v181", "v182", "v183", "v184", "v185", "v186", "v187", "v188", "v189", "v190",
        "v191", "v192", "v193", "v194", "v195", "v196", "v197", "v198", "v199", "v200", "v201", "v202", "v203", "v204", "v205", "v206",
        "v207", "v208", "v209", "v210", "v211", "v212", "v213", "v214", "v215", "v216", "v217", "v218", "v219", "v220", "v221", "v222",
        "v223", "v224", "v225", "v226", "v227", "v228", "v229", "v230", "v231", "v232", "v233", "v234", "v235", "v236", "v237", "v238",
        "v239", "v240", "v241", "v242", "v243", "v244", "v245", "v246", "v247", "v248", "v249", "v250", "v251", "v252", "v253", "v254",
        "v255", "a0", "a1", "a2", "a3", "a4", "a5", "a6", "a7", "a8", "a9", "a10", "a11", "a12", "a13", "a14",
        "a15", "a16", "a17", "a18", "a19", "a20", "a21", "a22", "a23", "a24", "a25", "a26", "a27", "a28", "a29", "a30",
        "a31", "a32", "a33", "a34", "a35", "a36", "a37", "a38", "a39", "a40", "a41", "a42", "a43", "a44", "a45", "a46",
        "a47", "a48", "a49", "a50", "a51", "a52", "a53", "a54", "a55", "a56", "a57", "a58", "a59", "a60", "a61", "a62",
        "a63", "a64", "a65", "a66", "a67", "a68", "a69", "a70", "a71", "a72", "a73", "a74", "a75", "a76", "a77", "a78",
        "a79", "a80", "a81", "a82", "a83", "a84", "a85", "a86", "a87", "a88", "a89", "a90", "a91", "a92", "a93", "a94",
        "a95", "a96", "a97", "a98", "a99", "a100", "a101", "a102", "a103", "a104", "a105", "a106", "a107", "a108", "a109", "a110",
        "a111", "a112", "a113", "a114", "a115", "a116", "a117", "a118", "a119", "a120", "a121", "a122", "a123", "a124", "a125", "a126",
        "a127", "a128", "a129", "a130", "a131", "a132", "a133", "a134", "a135", "a136", "a137", "a138", "a139", "a140", "a141", "a142",
        "a143", "a144", "a145", "a146", "a147", "a148", "a149", "a150", "a151", "a152", "a153", "a154", "a155", "a156", "a157", "a158",
        "a159", "a160", "a161", "a162", "a163", "a164", "a165", "a166", "a167", "a168", "a169", "a170", "a171", "a172", "a173", "a174",
        "a175", "a176", "a177", "a178", "a179", "a180", "a181", "a182", "a183", "a184", "a185", "a186", "a187", "a188", "a189", "a190",
        "a191", "a192", "a193", "a194", "a195", "a196", "a197", "a198", "a199", "a200", "a201", "a202", "a203", "a204", "a205", "a206",
        "a207", "a208", "a209", "a210", "a211", "a212", "a213", "a214", "a215", "a216", "a217", "a218", "a219", "a220", "a221", "a222",
        "a223", "a224", "a225", "a226", "a227", "a228", "a229", "a230", "a231", "a232", "a233", "a234", "a235", "a236", "a237", "a238",
        "a239", "a240", "a241", "a242", "a243", "a244", "a245", "a246", "a247", "a248", "a249", "a250", "a251", "a252", "a253", "a254",
        "a255");
}

extern "C" void kernel_launch(void* const* d_in, const int* in_sizes, int n_in, void* d_out, int out_size,
                              void* d_ws, size_t ws_size, hipStream_t stream) {
  const int lds = 163840;
  (void)hipFuncSetAttribute((const void*)dcn_cross_kernel, hipFuncAttributeMaxDynamicSharedMemorySize, lds);
  dcn_cross_kernel<<<256, 256, lds, stream>>>(d_in[0], d_in[1], d_in[2], d_out, d_ws);
}
```
